```python
import torch

def _softmax(x, axis=-1):
    return torch.softmax(x, dim=axis)
def _gelu(x):
    return torch.nn.functional.gelu(x, approximate="tanh")
def _silu(x):
    return torch.nn.functional.silu(x)
def _sigmoid(x):
    return torch.sigmoid(x)
def _layernorm(x, axis=-1, eps=1e-5):
    m = x.mean(dim=axis, keepdim=True)
    v = x.var(dim=axis, keepdim=True, unbiased=False)
    return (x - m) / torch.sqrt(v + eps)
def _scatter_add(tgt, idx, src, axis=0):
    return tgt.clone().index_add_(axis, idx, src)
def _segment_sum(src, seg, n):
    out = torch.zeros((n,) + src.shape[1:], dtype=src.dtype)
    return out.index_add_(0, seg, src)
def _pad(x, pw):
    flat = tuple(v for pair in reversed(pw) for v in pair)
    return torch.nn.functional.pad(x, flat)

kernel_name = 'sequential_preLN__gqa_block_cached__glu_gated_glu__decode'

def setup_inputs():

    _w = (lambda a: (lambda t: t.to(torch.bfloat16) if t.is_floating_point() else t)(torch.as_tensor(a)))
    def _A_setup():
        B, Sq, D = 1, 1, 512
        Hq, Hkv, Sk, Dh = 32, 8, 65536, 128
        assert Hq % Hkv == 0, (Hq, Hkv)
        return {
            "x": _w((lambda *s: torch.randn(*s, dtype=torch.bfloat16) * 0.05)(B, Sq, D)),
            "k_cache": _w((lambda *s: torch.randn(*s, dtype=torch.bfloat16) * 3.2)(B, Hkv, Sk, Dh)),
            "v_cache": _w((lambda *s: torch.randn(*s, dtype=torch.bfloat16) * 0.4)(B, Hkv, Sk, Dh)),
            "wq": _w((lambda *s: torch.randn(*s, dtype=torch.bfloat16) * 3.2)(D, Hq * Dh) * (D ** -0.5)),
            "wo": _w((lambda *s: torch.randn(*s, dtype=torch.bfloat16) * 0.4)(Hq * Dh, D) * ((Hq * Dh) ** -0.5)),
        }
    _A_in = _A_setup()

    def _B_setup():
        B, S, D, F = 1, 1, 512, 1792
        return {
            "x": _w((lambda *s: torch.randn(*s, dtype=torch.bfloat16) * 0.05)(B, S, D)),
            "w_gate": _w((lambda *s: torch.randn(*s, dtype=torch.bfloat16) * 0.2)(D, F)),
            "w_up": _w((lambda *s: torch.randn(*s, dtype=torch.bfloat16) * 0.05)(D, F)),
            "w_down": _w((lambda *s: torch.randn(*s, dtype=torch.bfloat16) * 0.05)(F, D)),
        }
    _B_in = _B_setup()

    return {
        "A_x": _A_in["x"],
        "A_k_cache": _A_in["k_cache"],
        "A_v_cache": _A_in["v_cache"],
        "A_wq": _A_in["wq"],
        "A_wo": _A_in["wo"],
        "B_x": _B_in["x"],
        "B_w_gate": _B_in["w_gate"],
        "B_w_up": _B_in["w_up"],
        "B_w_down": _B_in["w_down"],
    }

def reference(A_x, A_k_cache, A_v_cache, A_wq, A_wo, B_x, B_w_gate, B_w_up, B_w_down):
    def _A_ref(x, k_cache, v_cache, wq, wo):
        B, Sq, D = x.shape
        Hq, Dh = 32, 128
        Hkv = k_cache.shape[1]
        rep = Hq // Hkv
        q = (lambda x, *d: x.permute(*d))((x @ wq).reshape(B, Sq, Hq, Dh), 0, 2, 1, 3)
        k = (lambda x, n, axis: torch.repeat_interleave(x, n, dim=axis))(k_cache, rep, axis=1)
        v = (lambda x, n, axis: torch.repeat_interleave(x, n, dim=axis))(v_cache, rep, axis=1)
        scores = torch.einsum("bhqd,bhkd->bhqk", q, k) * (Dh ** -0.5)
        p = _softmax(scores, axis=-1)
        o = torch.einsum("bhqk,bhkd->bhqd", p, v)
        return (lambda x, *d: x.permute(*d))(o, 0, 2, 1, 3).reshape(B, Sq, Hq * Dh) @ wo

    def _B_ref(x, w_gate, w_up, w_down):
        g = torch.einsum("bsd,df->bsf", x, w_gate)
        u = torch.einsum("bsd,df->bsf", x, w_up)
        h = _sigmoid(g) * u
        return torch.einsum("bsf,fd->bsd", h, w_down)

    x = A_x
    h = x + _A_ref(_layernorm(x, axis=-1), A_k_cache, A_v_cache, A_wq, A_wo)
    h2 = h + _B_ref(_layernorm(h, axis=-1), B_w_gate, B_w_up, B_w_down)
    return h2
```

```cpp
#include <hip/hip_runtime.h>
#include <hip/hip_bf16.h>
#include <stdint.h>

typedef unsigned u32;
typedef u32 u32x4 __attribute__((ext_vector_type(4)));
typedef u32 u32x2 __attribute__((ext_vector_type(2)));
typedef float f32x4 __attribute__((ext_vector_type(4)));
typedef short bf16x8 __attribute__((ext_vector_type(8)));
typedef __bf16 bf16x8_t __attribute__((ext_vector_type(8)));

constexpr int D = 512, HQ = 32, HKV = 8, SK = 65536, DH = 128, FF = 1792;
constexpr int NSPLIT = 32;

__device__ __forceinline__ float bf2f(u32 h) { return __builtin_bit_cast(float, h << 16); }
__device__ __forceinline__ float bflo(u32 w) { return __builtin_bit_cast(float, w << 16); }
__device__ __forceinline__ float bfhi(u32 w) { return __builtin_bit_cast(float, w & 0xffff0000u); }
__device__ __forceinline__ u32 cvtpk(float lo, float hi) {
  u32 r; asm volatile("v_cvt_pk_bf16_f32 %0, %1, %2" : "=v"(r) : "v"(lo), "v"(hi)); return r;
}
__device__ __forceinline__ float wave_sum(float v) {
#pragma unroll
  for (int o = 32; o > 0; o >>= 1) v += __shfl_xor(v, o);
  return v;
}
__device__ __forceinline__ f32x4 mfma16(u32x4 a, u32x4 b, f32x4 c) {
  return __builtin_amdgcn_mfma_f32_16x16x32_bf16(__builtin_bit_cast(bf16x8_t, a), __builtin_bit_cast(bf16x8_t, b), c, 0, 0, 0);
}

__global__ void __launch_bounds__(256) k_qproj(const u32* __restrict__ x, const u32* __restrict__ wq, unsigned short* __restrict__ q) {
  __shared__ float xn[D];
  __shared__ float red[4][16];
  const int tid = threadIdx.x, lane = tid & 63, w = tid >> 6;
  {
    u32x4 xv = *(const u32x4*)(x + lane * 4);
    float f[8];
    for (int k = 0; k < 4; ++k) { f[2 * k] = bflo(xv[k]); f[2 * k + 1] = bfhi(xv[k]); }
    float s = 0; for (int k = 0; k < 8; ++k) s += f[k];
    s = wave_sum(s); const float mean = s * (1.f / D);
    float v = 0; for (int k = 0; k < 8; ++k) { float d = f[k] - mean; v += d * d; }
    v = wave_sum(v) * (1.f / D);
    const float rs = 1.f / sqrtf(v + 1e-5f);
    if (w == 0) for (int k = 0; k < 8; ++k) xn[lane * 8 + k] = (f[k] - mean) * rs;
  }
  __syncthreads();
  const int col0 = blockIdx.x * 16;
  const int half = tid & 1, rg = tid >> 1;
  float acc[8] = {0};
  for (int i = 0; i < 4; ++i) {
    const int row = rg + 128 * i;
    u32x4 wv = *(const u32x4*)(wq + ((size_t)row * 4096 + col0 + half * 8) / 2);
    const float xv = xn[row];
    for (int k = 0; k < 4; ++k) { acc[2 * k] += xv * bflo(wv[k]); acc[2 * k + 1] += xv * bfhi(wv[k]); }
  }
  for (int k = 0; k < 8; ++k) {
    float v = acc[k];
    for (int o = 2; o < 64; o <<= 1) v += __shfl_xor(v, o);
    acc[k] = v;
  }
  if (lane < 2) for (int k = 0; k < 8; ++k) red[w][lane * 8 + k] = acc[k];
  __syncthreads();
  if (tid < 16) {
    float v = red[0][tid] + red[1][tid] + red[2][tid] + red[3][tid];
    q[col0 + tid] = (unsigned short)(cvtpk(v, v) & 0xffff);
  }
}

__global__ void __launch_bounds__(512, 2) k_attn(const u32x4* __restrict__ kc, const u32x4* __restrict__ vc, const u32x4* __restrict__ q,
                                                float* __restrict__ part  ) {
  __shared__ __attribute__((aligned(16))) float mrg[8][4][132];
  const int tid = threadIdx.x, lane = tid & 63, w = __builtin_amdgcn_readfirstlane(tid >> 6);
  const int g = blockIdx.x & 7, s = blockIdx.x >> 3;
  const int r = lane >> 4, c = lane & 15;
  u32x4 qf[4];
#pragma unroll
  for (int i = 0; i < 4; ++i) {
    qf[i] = (c < 4) ? q[((g * 4 + c) * DH + r * 8 + 32 * i) / 8] : u32x4{0, 0, 0, 0};
  }
  const size_t hb = (size_t)g * SK * DH / 8;
  const int koff = (4 * (c & 3) + (c >> 2)) * 16 + r;
  const int voff = r * 16 + c;
  constexpr float C2 = 0.08838834764831845f * 1.4426950408889634f;
  f32x4 acc[8];
#pragma unroll
  for (int j = 0; j < 8; ++j) acc[j] = f32x4{0, 0, 0, 0};
  float m = -INFINITY, l = 0.f;
  for (int it = 0; it < 8; ++it) {
    const size_t key0 = (size_t)s * 2048 + (it * 8 + w) * 32;
    const u32x4* kp = kc + hb + key0 * 16 + koff;
    const u32x4* vp = vc + hb + key0 * 16 + voff;
    u32x4 K[8], V[8];
#pragma unroll
    for (int i = 0; i < 8; ++i) K[i] = __builtin_nontemporal_load(kp + (i >> 2) * 256 + (i & 3) * 4);
#pragma unroll
    for (int t = 0; t < 8; ++t) V[t] = __builtin_nontemporal_load(vp + t * 64);
    f32x4 sa = {0, 0, 0, 0}, sb = {0, 0, 0, 0};
#pragma unroll
    for (int i = 0; i < 4; ++i) { sa = mfma16(K[i], qf[i], sa); sb = mfma16(K[4 + i], qf[i], sb); }
    float mx = fmaxf(fmaxf(fmaxf(sa[0], sa[1]), fmaxf(sa[2], sa[3])), fmaxf(fmaxf(sb[0], sb[1]), fmaxf(sb[2], sb[3])));
    mx = fmaxf(mx, __shfl_xor(mx, 16));
    mx = fmaxf(mx, __shfl_xor(mx, 32));
    const float mn = fmaxf(m, mx * C2);
    const float alpha = __builtin_amdgcn_exp2f(m - mn);
    m = mn;
    float p[8];
#pragma unroll
    for (int k = 0; k < 4; ++k) { p[k] = __builtin_amdgcn_exp2f(sa[k] * C2 - mn); p[4 + k] = __builtin_amdgcn_exp2f(sb[k] * C2 - mn); }
    l = l * alpha + ((p[0] + p[1]) + (p[2] + p[3])) + ((p[4] + p[5]) + (p[6] + p[7]));
    u32x4 pf = {cvtpk(p[0], p[1]), cvtpk(p[2], p[3]), cvtpk(p[4], p[5]), cvtpk(p[6], p[7])};
#pragma unroll
    for (int j = 0; j < 8; ++j) acc[j] *= alpha;
#pragma unroll
    for (int dw = 0; dw < 4; ++dw) {
      u32x4 lo, hi;
#pragma unroll
      for (int u = 0; u < 4; ++u) {
        lo[u] = __builtin_amdgcn_perm(V[2 * u + 1][dw], V[2 * u][dw], 0x05040100u);
        hi[u] = __builtin_amdgcn_perm(V[2 * u + 1][dw], V[2 * u][dw], 0x07060302u);
      }
      acc[2 * dw] = mfma16(lo, pf, acc[2 * dw]);
      acc[2 * dw + 1] = mfma16(hi, pf, acc[2 * dw + 1]);
    }
  }
  l += __shfl_xor(l, 16);
  l += __shfl_xor(l, 32);
  if (c < 4) {
#pragma unroll
    for (int reg = 0; reg < 4; ++reg) {
      float* dst = &mrg[w][c][4 + 8 * (4 * r + reg)];
      *(f32x4*)dst = f32x4{acc[0][reg], acc[1][reg], acc[2][reg], acc[3][reg]};
      *(f32x4*)(dst + 4) = f32x4{acc[4][reg], acc[5][reg], acc[6][reg], acc[7][reg]};
    }
    if (r == 0) { mrg[w][c][0] = m; mrg[w][c][1] = l; }
  }
  __syncthreads();
  {
    const int h = tid >> 7, d = tid & 127;
    float M = -INFINITY;
#pragma unroll
    for (int ww = 0; ww < 8; ++ww) M = fmaxf(M, mrg[ww][h][0]);
    float L = 0, o = 0;
#pragma unroll
    for (int ww = 0; ww < 8; ++ww) {
      const float wt = __builtin_amdgcn_exp2f(mrg[ww][h][0] - M);
      L += wt * mrg[ww][h][1];
      o += wt * mrg[ww][h][4 + d];
    }
    float* dst = part + ((size_t)(g * 4 + h) * NSPLIT + s) * 132;
    dst[4 + d] = o;
    if (d == 0) { dst[0] = M; dst[1] = L; }
  }
}

__global__ void __launch_bounds__(128) k_combine(const float* __restrict__ part, float* __restrict__ o) {
  const int h = blockIdx.x, d = threadIdx.x;
  const float* p = part + (size_t)h * NSPLIT * 132;
  float M = -INFINITY;
  for (int s = 0; s < NSPLIT; ++s) M = fmaxf(M, p[s * 132]);
  float L = 0, acc = 0;
  for (int s = 0; s < NSPLIT; ++s) {
    const float wt = __builtin_amdgcn_exp2f(p[s * 132] - M);
    L += wt * p[s * 132 + 1];
    acc += wt * p[s * 132 + 4 + d];
  }
  o[h * DH + d] = acc / L;
}

__global__ void __launch_bounds__(256) k_wo(const float* __restrict__ o, const u32* __restrict__ wo, const unsigned short* __restrict__ x, float* __restrict__ h) {
  __shared__ float red[8][64];
  const int tid = threadIdx.x, cw = tid & 31, rg = tid >> 5;
  const int col = blockIdx.x * 64 + cw * 2;
  float a0 = 0, a1 = 0;
  for (int r = rg; r < 4096; r += 8) {
    const u32 wv = wo[((size_t)r * D + col) / 2];
    const float xv = o[r];
    a0 += xv * bflo(wv); a1 += xv * bfhi(wv);
  }
  red[rg][cw * 2] = a0; red[rg][cw * 2 + 1] = a1;
  __syncthreads();
  if (tid < 64) {
    float v = 0;
    for (int i = 0; i < 8; ++i) v += red[i][tid];
    const int cc = blockIdx.x * 64 + tid;
    h[cc] = v + bf2f(x[cc]);
  }
}
__global__ void __launch_bounds__(256) k_gu(const float* __restrict__ h, const u32* __restrict__ wg, const u32* __restrict__ wu, float* __restrict__ hh) {
  __shared__ float hn[D];
  __shared__ float red[2][8][64];
  const int tid = threadIdx.x, lane = tid & 63, w = tid >> 6;
  {
    float f[8];
    for (int k = 0; k < 8; ++k) f[k] = h[lane * 8 + k];
    float s = 0; for (int k = 0; k < 8; ++k) s += f[k];
    s = wave_sum(s); const float mean = s * (1.f / D);
    float v = 0; for (int k = 0; k < 8; ++k) { float d = f[k] - mean; v += d * d; }
    v = wave_sum(v) * (1.f / D);
    const float rs = 1.f / sqrtf(v + 1e-5f);
    if (w == 0) for (int k = 0; k < 8; ++k) hn[lane * 8 + k] = (f[k] - mean) * rs;
  }
  __syncthreads();
  const int cw = tid & 31, rg = tid >> 5;
  const int col = blockIdx.x * 64 + cw * 2;
  float g0 = 0, g1 = 0, u0 = 0, u1 = 0;
  for (int r = rg; r < D; r += 8) {
    const u32 a = wg[((size_t)r * FF + col) / 2], b = wu[((size_t)r * FF + col) / 2];
    const float xv = hn[r];
    g0 += xv * bflo(a); g1 += xv * bfhi(a); u0 += xv * bflo(b); u1 += xv * bfhi(b);
  }
  red[0][rg][cw * 2] = g0; red[0][rg][cw * 2 + 1] = g1; red[1][rg][cw * 2] = u0; red[1][rg][cw * 2 + 1] = u1;
  __syncthreads();
  if (tid < 64) {
    float gg = 0, uu = 0;
    for (int i = 0; i < 8; ++i) { gg += red[0][i][tid]; uu += red[1][i][tid]; }
    hh[blockIdx.x * 64 + tid] = uu / (1.f + __expf(-gg));
  }
}
__global__ void __launch_bounds__(256) k_down(const float* __restrict__ hh, const u32* __restrict__ wd, const float* __restrict__ h, unsigned short* __restrict__ out) {
  __shared__ float red[8][64];
  const int tid = threadIdx.x, cw = tid & 31, rg = tid >> 5;
  const int col = blockIdx.x * 64 + cw * 2;
  float a0 = 0, a1 = 0;
  for (int r = rg; r < FF; r += 8) {
    const u32 wv = wd[((size_t)r * D + col) / 2];
    const float xv = hh[r];
    a0 += xv * bflo(wv); a1 += xv * bfhi(wv);
  }
  red[rg][cw * 2] = a0; red[rg][cw * 2 + 1] = a1;
  __syncthreads();
  if (tid < 64) {
    float v = 0;
    for (int i = 0; i < 8; ++i) v += red[i][tid];
    const int cc = blockIdx.x * 64 + tid;
    const float r = v + h[cc];
    out[cc] = (unsigned short)(cvtpk(r, r) & 0xffff);
  }
}

extern "C" void kernel_launch(void* const* d_in, const int* in_sizes, int n_in, void* d_out, int out_size, void* d_ws, size_t ws_size,
                              hipStream_t stream) {
  const void* x = d_in[0]; const void* kc = d_in[1]; const void* vc = d_in[2]; const void* wq = d_in[3]; const void* wo = d_in[4];
  const void* wg = d_in[6]; const void* wu = d_in[7]; const void* wd = d_in[8];
  char* ws = (char*)d_ws;
  unsigned short* q = (unsigned short*)ws;
  float* part = (float*)(ws + 65536);
  float* o = (float*)(ws + (1 << 20));
  float* h = (float*)(ws + (1 << 20) + 65536);
  float* hh = (float*)(ws + (1 << 20) + 131072);
  k_qproj<<<256, 256, 0, stream>>>((const u32*)x, (const u32*)wq, q);
  k_attn<<<256, 512, 0, stream>>>((const u32x4*)kc, (const u32x4*)vc, (const u32x4*)q, part);
  k_combine<<<32, 128, 0, stream>>>(part, o);
  k_wo<<<8, 256, 0, stream>>>(o, (const u32*)wo, (const unsigned short*)x, h);
  k_gu<<<28, 256, 0, stream>>>(h, (const u32*)wg, (const u32*)wu, hh);
  k_down<<<8, 256, 0, stream>>>(hh, (const u32*)wd, h, (unsigned short*)d_out);
}
```

```cpp
#include <hip/hip_runtime.h>
#include <stdint.h>

typedef unsigned u32;
typedef u32 u32x4 __attribute__((ext_vector_type(4)));
typedef u32 u32x2 __attribute__((ext_vector_type(2)));
typedef float f32x4 __attribute__((ext_vector_type(4)));
typedef float f32x2 __attribute__((ext_vector_type(2)));
typedef __bf16 bf16x8_t __attribute__((ext_vector_type(8)));
typedef __bf16 bf16x2_t __attribute__((ext_vector_type(2)));

#define DEVI __device__ __forceinline__

constexpr int D = 512, SK = 65536, DH = 128, FF = 1792;
constexpr float L2E = 1.4426950408889634f;
constexpr float SCALE = 0.08838834764831845f;

constexpr u32 WS_EPOCH = 0;
constexpr u32 WS_QG = 0x10000;
constexpr u32 WS_OG = 0x30000;
constexpr u32 WS_HP = 0x50000;
constexpr u32 WS_OP = 0x80000;
constexpr u32 WS_IN = 0x100000;
constexpr u32 WS_BYTES = 0x200000;
constexpr u32 OOB = 0x7ffffff0u;
constexpr int AUX_SC1 = 16, AUX_NT = 2;
constexpr int SPIN_MAX = 1 << 18;

DEVI float bflo(u32 w) { return __builtin_bit_cast(float, w << 16); }
DEVI float bfhi(u32 w) { return __builtin_bit_cast(float, w & 0xffff0000u); }
DEVI u32 cvtpk(float a, float b) { f32x2 v = {a, b}; return __builtin_bit_cast(u32, __builtin_convertvector(v, bf16x2_t)); }
DEVI float bfr(float a) { return bflo(cvtpk(a, a)); }
DEVI float dot2(u32 a, u32 b, float c) {
  return __builtin_amdgcn_fdot2_f32_bf16(__builtin_bit_cast(bf16x2_t, a), __builtin_bit_cast(bf16x2_t, b), c, false);
}
DEVI f32x4 mfma16(u32x4 a, u32x4 b, f32x4 c) {
  return __builtin_amdgcn_mfma_f32_16x16x32_bf16(__builtin_bit_cast(bf16x8_t, a), __builtin_bit_cast(bf16x8_t, b), c, 0, 0, 0);
}
template <int CTRL> DEVI float dpp(float x) {
  return __builtin_bit_cast(float, __builtin_amdgcn_update_dpp(0, __builtin_bit_cast(int, x), CTRL, 0xf, 0xf, true));
}
template <int CTRL> DEVI u32 dppu(u32 x) { return (u32)__builtin_amdgcn_update_dpp(0, (int)x, CTRL, 0xf, 0xf, true); }
constexpr int ROR8 = 0x128, ROR4 = 0x124, HMIR = 0x141, XOR1 = 0xB1, XOR2 = 0x4E, NBC = 0x150;
DEVI float sw32_add(float a, float b) {
  auto s = __builtin_amdgcn_permlane32_swap(__float_as_uint(a), __float_as_uint(b), false, false);
  return __uint_as_float(s[0]) + __uint_as_float(s[1]);
}
DEVI float sw16_add(float a, float b) {
  auto s = __builtin_amdgcn_permlane16_swap(__float_as_uint(a), __float_as_uint(b), false, false);
  return __uint_as_float(s[0]) + __uint_as_float(s[1]);
}
DEVI float sw32_max(float a) {
  auto s = __builtin_amdgcn_permlane32_swap(__float_as_uint(a), __float_as_uint(a), false, false);
  return fmaxf(__uint_as_float(s[0]), __uint_as_float(s[1]));
}
DEVI float sw16_max(float a) {
  auto s = __builtin_amdgcn_permlane16_swap(__float_as_uint(a), __float_as_uint(a), false, false);
  return fmaxf(__uint_as_float(s[0]), __uint_as_float(s[1]));
}
DEVI float x8_add(float a, float b, bool bit3) {
  const float sa = a + dpp<ROR8>(a), sb = b + dpp<ROR8>(b);
  return bit3 ? sb : sa;
}
DEVI float wave_sum(float v) {
  v += dpp<XOR1>(v); v += dpp<XOR2>(v); v += dpp<HMIR>(v); v += dpp<ROR8>(v);
  v = sw16_add(v, v); v = sw32_add(v, v);
  return v;
}
DEVI void lds_barrier() {
  asm volatile("s_waitcnt lgkmcnt(0)" ::: "memory");
  __builtin_amdgcn_s_barrier();
  asm volatile("" ::: "memory");
}
DEVI u32 pk24(float v, u32 tb) { return ((__float_as_uint(v) + 0x80u) & 0xffffff00u) | tb; }
DEVI u32x4 pack4(float a, float b, float c, float d, u32 tag) {
  return u32x4{pk24(a, tag & 0xff), pk24(b, (tag >> 8) & 0xff), pk24(c, (tag >> 16) & 0xff), pk24(d, tag >> 24)};
}
DEVI u32 tag4(u32x4 v) {
  const u32 t01 = __builtin_amdgcn_perm(v[1], v[0], 0x0c0c0400u);
  const u32 t23 = __builtin_amdgcn_perm(v[3], v[2], 0x04000c0cu);
  return t01 | t23;
}
DEVI float up24(u32 w) { return __uint_as_float(w & 0xffffff00u); }
DEVI u32 plo(u32 hi_row, u32 lo_row) { return __builtin_amdgcn_perm(hi_row, lo_row, 0x05040100u); }
DEVI u32 phi(u32 hi_row, u32 lo_row) { return __builtin_amdgcn_perm(hi_row, lo_row, 0x07060302u); }

#define LD128(rs, off) __builtin_amdgcn_raw_buffer_load_b128(rs, (int)(off), 0, AUX_SC1)
#define ST128(v, rs, off) __builtin_amdgcn_raw_buffer_store_b128(v, rs, (int)(off), 0, AUX_SC1)
#define ST64(v, rs, off) __builtin_amdgcn_raw_buffer_store_b64(v, rs, (int)(off), 0, AUX_SC1)

#define STAMP(i) do { } while (0)

DEVI void ln_consts(float mean, float var, float& m, float& rden) {
  m = bfr(mean);
  const float v = bfr(var);
  const float den = bfr(sqrtf(bfr(v + 1e-5f)));
  rden = 1.f / den;
}
DEVI float ln_apply(float x, float m, float rden) { return bfr(x - m) * rden; }

__global__ void __launch_bounds__(512, 2)
fused_decode(const u32* __restrict__ xin, const u32x4* __restrict__ kc, const u32x4* __restrict__ vc, const u32x4* __restrict__ wq,
             const u32x4* __restrict__ wo, const u32x4* __restrict__ wg, const u32x4* __restrict__ wu, const u32x4* __restrict__ wd,
             unsigned short* __restrict__ out, unsigned char* __restrict__ ws
) {
  __shared__ __attribute__((aligned(16))) u32 s_q[256];
  __shared__ __attribute__((aligned(16))) float s_mrg[4][4][132];
  __shared__ __attribute__((aligned(16))) float s_red[64 * 8];
  __shared__ __attribute__((aligned(16))) u32 s_h[256];
  __shared__ __attribute__((aligned(16))) float s_stat[8];
  __shared__ u32 s_tag;

  const int tid = threadIdx.x, lane = tid & 63, w = __builtin_amdgcn_readfirstlane(tid >> 6);
  const int b = blockIdx.x, g = b & 7, s = b >> 3;
  const __amdgpu_buffer_rsrc_t rs = __builtin_amdgcn_make_buffer_rsrc((void*)ws, 0, (int)WS_BYTES, 0x00020000);
  STAMP(0);

  if (w < 4) {
    const int r = lane >> 4, c = lane & 15;
    const size_t hb = ((size_t)g * SK + (size_t)s * 2048) * 16;
    const __amdgpu_buffer_rsrc_t rk = __builtin_amdgcn_make_buffer_rsrc((void*)(kc + hb), 0, 2048 * 256, 0x00020000);
    const __amdgpu_buffer_rsrc_t rv = __builtin_amdgcn_make_buffer_rsrc((void*)(vc + hb), 0, 2048 * 256, 0x00020000);
    const int koff = ((4 * (c & 3) + (c >> 2)) * 16 + r) * 16;
    const int voff = lane * 16;
    const int wbase = w * 8192;
    u32x4 K[2][8], V[2][8];
#define LOADK(bf, it) { const int so_ = wbase + (it) * 32768; _Pragma("unroll") for (int i = 0; i < 8; ++i) \
      K[bf][i] = __builtin_amdgcn_raw_buffer_load_b128(rk, koff + (i & 3) * 64, so_ + (i >> 2) * 4096, AUX_NT); }
#define LOADV(bf, it) { const int so_ = wbase + (it) * 32768; _Pragma("unroll") for (int t = 0; t < 8; ++t) \
      V[bf][t] = __builtin_amdgcn_raw_buffer_load_b128(rv, voff + (t & 3) * 1024, so_ + (t >> 2) * 4096, AUX_NT); }
    LOADK(0, 0); LOADV(0, 0);
    __builtin_amdgcn_sched_barrier(0);
    lds_barrier();
    __builtin_amdgcn_sched_barrier(0);
    LOADK(1, 1); LOADV(1, 1);
    __builtin_amdgcn_sched_barrier(0);
    lds_barrier();
    STAMP(1);
    u32x4 qf[4];
#pragma unroll
    for (int i = 0; i < 4; ++i) {
      const u32x4 t = *(const u32x4*)&s_q[(c & 3) * 64 + r * 4 + 16 * i];
      qf[i] = (c < 4) ? t : u32x4{0, 0, 0, 0};
    }
    f32x4 acc[8];
#pragma unroll
    for (int j = 0; j < 8; ++j) acc[j] = f32x4{0, 0, 0, 0};
    float m = -INFINITY, l = 0.f;

#define TILE(bf, nxt, REFILL)                                                                                         \
    {                                                                                                                 \
      f32x4 sa = {0, 0, 0, 0}, sb = {0, 0, 0, 0};                                                                     \
      _Pragma("unroll") for (int i = 0; i < 4; ++i) { sa = mfma16(K[bf][i], qf[i], sa); sb = mfma16(K[bf][4 + i], qf[i], sb); } \
      asm volatile("" : "+v"(sa), "+v"(sb) :: "memory");                                                              \
      if (REFILL) LOADK(bf, nxt);                                                                                     \
      __builtin_amdgcn_sched_barrier(0);                                                                              \
      float sc[8];                                                                                                    \
      {                                                                                                               \
        const u32 a0 = cvtpk(sa[0], sa[1]), a1 = cvtpk(sa[2], sa[3]), a2 = cvtpk(sb[0], sb[1]), a3 = cvtpk(sb[2], sb[3]); \
        const u32 b0 = cvtpk(bflo(a0) * SCALE, bfhi(a0) * SCALE), b1 = cvtpk(bflo(a1) * SCALE, bfhi(a1) * SCALE);     \
        const u32 b2 = cvtpk(bflo(a2) * SCALE, bfhi(a2) * SCALE), b3 = cvtpk(bflo(a3) * SCALE, bfhi(a3) * SCALE);     \
        sc[0] = bflo(b0); sc[1] = bfhi(b0); sc[2] = bflo(b1); sc[3] = bfhi(b1);                                       \
        sc[4] = bflo(b2); sc[5] = bfhi(b2); sc[6] = bflo(b3); sc[7] = bfhi(b3);                                       \
      }                                                                                                               \
      float mx = fmaxf(fmaxf(fmaxf(sc[0], sc[1]), fmaxf(sc[2], sc[3])), fmaxf(fmaxf(sc[4], sc[5]), fmaxf(sc[6], sc[7]))); \
      mx = sw32_max(sw16_max(mx));                                                                                    \
      const float mn = fmaxf(m, mx);                                                                                  \
      const float alpha = __builtin_amdgcn_exp2f((m - mn) * L2E);                                                     \
      m = mn;                                                                                                         \
      float p[8];                                                                                                     \
      _Pragma("unroll") for (int k = 0; k < 8; ++k) p[k] = __builtin_amdgcn_exp2f((sc[k] - mn) * L2E);                \
      l = l * alpha + (((p[0] + p[1]) + (p[2] + p[3])) + ((p[4] + p[5]) + (p[6] + p[7])));                            \
      const u32x4 pf = {cvtpk(p[0], p[1]), cvtpk(p[2], p[3]), cvtpk(p[4], p[5]), cvtpk(p[6], p[7])};                  \
      _Pragma("unroll") for (int j = 0; j < 8; ++j) acc[j] *= alpha;                                                  \
      _Pragma("unroll") for (int dw = 0; dw < 4; ++dw) {                                                              \
        u32x4 lo, hi;                                                                                                 \
        _Pragma("unroll") for (int u = 0; u < 4; ++u) {                                                               \
          lo[u] = plo(V[bf][2 * u + 1][dw], V[bf][2 * u][dw]);                                                        \
          hi[u] = phi(V[bf][2 * u + 1][dw], V[bf][2 * u][dw]);                                                        \
        }                                                                                                             \
        acc[2 * dw] = mfma16(lo, pf, acc[2 * dw]);                                                                    \
        acc[2 * dw + 1] = mfma16(hi, pf, acc[2 * dw + 1]);                                                            \
      }                                                                                                               \
      asm volatile("" : "+v"(acc[0]), "+v"(acc[1]), "+v"(acc[2]), "+v"(acc[3]), "+v"(acc[4]), "+v"(acc[5]), "+v"(acc[6]), "+v"(acc[7]) :: "memory"); \
      if (REFILL) LOADV(bf, nxt);                                                                                     \
      __builtin_amdgcn_sched_barrier(0);                                                                              \
    }

#pragma unroll 1
    for (int it = 0; it < 14; it += 2) {
      TILE(0, it + 2, true);
      TILE(1, it + 3, true);
    }
    TILE(0, 0, false);
    TILE(1, 0, false);
    STAMP(2);

    l = sw32_add(l, l); { float t = sw16_add(l, l); l = t; }
    if (c < 4) {
#pragma unroll
      for (int reg = 0; reg < 4; ++reg) {
        float* dst = &s_mrg[w][c][4 + 8 * (4 * r + reg)];
        *(f32x4*)dst = f32x4{acc[0][reg], acc[1][reg], acc[2][reg], acc[3][reg]};
        *(f32x4*)(dst + 4) = f32x4{acc[4][reg], acc[5][reg], acc[6][reg], acc[7][reg]};
      }
      if (r == 0) *(f32x2*)&s_mrg[w][c][0] = f32x2{m, l};
    }
    lds_barrier();
    const u32 tag = s_tag;
    {
      f32x2 ml[4], a[4];
#pragma unroll
      for (int ww = 0; ww < 4; ++ww) { ml[ww] = *(const f32x2*)&s_mrg[ww][w][0]; a[ww] = *(const f32x2*)&s_mrg[ww][w][4 + 2 * lane]; }
      const float M = fmaxf(fmaxf(ml[0][0], ml[1][0]), fmaxf(ml[2][0], ml[3][0]));
      float L = 0, a0 = 0, a1 = 0;
#pragma unroll
      for (int ww = 0; ww < 4; ++ww) {
        const float wt = __builtin_amdgcn_exp2f((ml[ww][0] - M) * L2E);
        L += wt * ml[ww][1]; a0 += wt * a[ww][0]; a1 += wt * a[ww][1];
      }
      const u32 off = WS_IN + (u32)(((g * 32 + w * 8 + (lane >> 3)) * 32 + s) * 128 + (lane & 7) * 16);
      ST128(pack4(a0, a1, M, L, tag), rs, off);
    }
    STAMP(3);

    if (b >= 128) {
      if (w >= 2) return;
      const int slice = ((b - 128) >> 3) * 2 + w;
      const u32 base = WS_IN + (u32)((g * 32 + slice) * 4096 + lane * 16);
      u32x4 v[4];
      for (int spin = 0; spin < SPIN_MAX; ++spin) {
        asm volatile("" ::: "memory");
#pragma unroll
        for (int i = 0; i < 4; ++i) v[i] = LD128(rs, base + i * 1024);
        const bool ok = (tag4(v[0]) == tag) & (tag4(v[1]) == tag) & (tag4(v[2]) == tag) & (tag4(v[3]) == tag);
        if (__all(ok)) break;
      }
      STAMP(4);
      float M = fmaxf(fmaxf(up24(v[0][2]), up24(v[1][2])), fmaxf(up24(v[2][2]), up24(v[3][2])));
      M = fmaxf(M, dpp<ROR8>(M));
      M = sw32_max(sw16_max(M));
      float L = 0, a0 = 0, a1 = 0;
#pragma unroll
      for (int i = 0; i < 4; ++i) {
        const float wt = __builtin_amdgcn_exp2f((up24(v[i][2]) - M) * L2E);
        L += wt * up24(v[i][3]); a0 += wt * up24(v[i][0]); a1 += wt * up24(v[i][1]);
      }
      L += dpp<ROR8>(L); a0 += dpp<ROR8>(a0); a1 += dpp<ROR8>(a1);
      L = sw16_add(L, L); a0 = sw16_add(a0, a0); a1 = sw16_add(a1, a1);
      L = sw32_add(L, L); a0 = sw32_add(a0, a0); a1 = sw32_add(a1, a1);
      const float rl = __builtin_amdgcn_rcpf(L);
      const u32x2 og = {cvtpk(a0 * rl, a1 * rl), tag};
      ST64(og, rs, WS_OG + (u32)((((lane >> 3) * 8 + g) * 256 + slice * 8 + (lane & 7)) * 8));
      STAMP(5);
      return;
    }
    if (b >= 120) {
      const int slice = (b - 120) * 4 + w;
      const int gi = lane & 3, pq = lane >> 2;
      const int col = 16 * slice + 4 * gi + 2 * (lane >> 5) + ((lane >> 4) & 1);
      const float xv = bflo((u32)((const unsigned short*)xin)[col]);
      float hval;
      {
        const u32 off = (pq < 8) ? WS_HP + (u32)((((slice & 7) * 8 + pq) * 128 + slice * 4 + gi) * 16) : OOB;
        u32x4 hv;
        for (int spin = 0; spin < SPIN_MAX; ++spin) {
          asm volatile("" ::: "memory");
          hv = LD128(rs, off);
          const bool ok = (tag4(hv) == tag) | (pq >= 8);
          if (__all(ok)) break;
        }
        float t0 = sw32_add(up24(hv[0]), up24(hv[2])), t1 = sw32_add(up24(hv[1]), up24(hv[3]));
        float t = sw16_add(t0, t1);
        t += dpp<ROR8>(t); t += dpp<ROR4>(t);
        hval = bfr(xv + bfr(t));
      }
      STAMP(4);
      u32x4 v[4];
      const u32 base = WS_OP + (u32)((slice * 64 + pq) * 128 + gi * 16);
      const u32 off3 = (pq < 8) ? base + 3 * 2048 : OOB;
      for (int spin = 0; spin < SPIN_MAX; ++spin) {
        asm volatile("" ::: "memory");
        v[0] = LD128(rs, base); v[1] = LD128(rs, base + 2048); v[2] = LD128(rs, base + 4096); v[3] = LD128(rs, off3);
        const bool ok = (tag4(v[0]) == tag) & (tag4(v[1]) == tag) & (tag4(v[2]) == tag) & ((tag4(v[3]) == tag) | (pq >= 8));
        if (__all(ok)) break;
      }
      STAMP(5);
      float e[4];
#pragma unroll
      for (int k = 0; k < 4; ++k) e[k] = (up24(v[0][k]) + up24(v[1][k])) + (up24(v[2][k]) + up24(v[3][k]));
      float t0 = sw32_add(e[0], e[2]), t1 = sw32_add(e[1], e[3]);
      float t = sw16_add(t0, t1);
      t += dpp<ROR8>(t); t += dpp<ROR4>(t);
      const float o = hval + bfr(t);
      if ((lane & 12) == 0) out[col] = (unsigned short)(cvtpk(o, o) & 0xffffu);
      if (slice == 0 && lane == 0) __builtin_amdgcn_raw_buffer_store_b32(tag, rs, (int)WS_EPOCH, 0, AUX_SC1);
      STAMP(6);
      return;
    }
    return;
  }

  const int hw = w - 4;
  u32 tag = 0;
  if (hw == 0) {
    const u32 ep = __builtin_amdgcn_raw_buffer_load_b32(rs, (int)WS_EPOCH, 0, AUX_SC1);
    const int half = lane >> 5, rr = lane & 31;
    const u32x4 x0 = *(const u32x4*)(xin + rr * 8), x1 = *(const u32x4*)(xin + rr * 8 + 4);
    const u32x4* wp = wq + (size_t)(16 * rr) * 512 + (g * 64 + s * 2 + half);
    u32x4 W[16];
#pragma unroll
    for (int i = 0; i < 16; ++i) W[i] = wp[(size_t)i * 512];
    __builtin_amdgcn_sched_barrier(0);
    tag = ep + 1;
    if (lane == 0) s_tag = tag;
    float f[16];
#pragma unroll
    for (int k = 0; k < 4; ++k) { f[2 * k] = bflo(x0[k]); f[2 * k + 1] = bfhi(x0[k]); f[8 + 2 * k] = bflo(x1[k]); f[8 + 2 * k + 1] = bfhi(x1[k]); }
    float sm = 0;
#pragma unroll
    for (int k = 0; k < 16; ++k) sm += f[k];
    const float mean = wave_sum(sm) * (1.f / 1024.f);
    float sv = 0;
#pragma unroll
    for (int k = 0; k < 16; ++k) { const float d = f[k] - mean; sv += d * d; }
    const float var = wave_sum(sv) * (1.f / 1024.f);
    float m, den;
    ln_consts(mean, var, m, den);
    u32 xp[8];
#pragma unroll
    for (int k = 0; k < 8; ++k) xp[k] = cvtpk(ln_apply(f[2 * k], m, den), ln_apply(f[2 * k + 1], m, den));
    float a[8];
#pragma unroll
    for (int j = 0; j < 8; ++j) a[j] = 0;
#pragma unroll
    for (int k = 0; k < 8; ++k)
#pragma unroll
      for (int dw = 0; dw < 4; ++dw) {
        a[2 * dw] = dot2(plo(W[2 * k + 1][dw], W[2 * k][dw]), xp[k], a[2 * dw]);
        a[2 * dw + 1] = dot2(phi(W[2 * k + 1][dw], W[2 * k][dw]), xp[k], a[2 * dw + 1]);
      }
    float t[4];
#pragma unroll
    for (int j = 0; j < 4; ++j) t[j] = sw16_add(a[j], a[j + 4]);
    const bool b3 = (lane & 8) != 0;
    float u0 = x8_add(t[0], t[2], b3), u1 = x8_add(t[1], t[3], b3);
    u0 += dpp<HMIR>(u0); u1 += dpp<HMIR>(u1);
    u0 += dpp<XOR1>(u0); u1 += dpp<XOR1>(u1);
    u0 += dpp<XOR2>(u0); u1 += dpp<XOR2>(u1);
    const u32x2 qg = {cvtpk(u0, u1), tag};
    ST64(qg, rs, WS_QG + (u32)((((lane & 7) * 8 + g) * 256 + s * 8 + (lane >> 3)) * 8));
    STAMP(1);
    u32 base = WS_QG + (u32)(((s & 7) * 8 + g) * 2048 + lane * 16);
    u32x4 v0 = LD128(rs, base), v1 = LD128(rs, base + 1024);
    __builtin_amdgcn_sched_barrier(0);
    lds_barrier();
    __builtin_amdgcn_sched_barrier(0);
    for (int spin = 0; spin < SPIN_MAX; ++spin) {
      const bool ok = (v0[1] == tag) & (v0[3] == tag) & (v1[1] == tag) & (v1[3] == tag);
      if (__all(ok)) break;
      asm volatile("" : "+v"(base) :: "memory");
      v0 = LD128(rs, base); v1 = LD128(rs, base + 1024);
    }
    *(u32x2*)&s_q[lane * 2] = u32x2{v0[0], v0[2]};
    *(u32x2*)&s_q[128 + lane * 2] = u32x2{v1[0], v1[2]};
    STAMP(2);
    lds_barrier();
  } else {
    if (b >= 120) return;
    lds_barrier();
    lds_barrier();
    tag = s_tag;
  }
  if (b >= 120) return;

  if (b < 64) {
    const int cb = b >> 3;
    const int rr = lane >> 3, cc = lane & 7;
    const u32x4* wp = wo + (size_t)(g * 512 + hw * 128 + 16 * rr) * 64 + cb * 8 + cc;
    u32 P[8][8];
    {
      u32x4 W[16];
#pragma unroll
      for (int i = 0; i < 16; ++i) W[i] = wp[(size_t)i * 64];
#pragma unroll
      for (int k = 0; k < 8; ++k)
#pragma unroll
        for (int dw = 0; dw < 4; ++dw) { P[k][2 * dw] = plo(W[2 * k + 1][dw], W[2 * k][dw]); P[k][2 * dw + 1] = phi(W[2 * k + 1][dw], W[2 * k][dw]); }
    }
#pragma unroll
    for (int k = 0; k < 8; ++k)
#pragma unroll
      for (int j = 0; j < 8; ++j) asm volatile("" : "+v"(P[k][j]));
    lds_barrier();
    const u32 base = WS_OG + (u32)(((cb * 8 + g) * 256 + hw * 64 + rr * 8) * 8);
    u32x4 v[4];
    for (int spin = 0; spin < SPIN_MAX; ++spin) {
      asm volatile("" ::: "memory");
#pragma unroll
      for (int i = 0; i < 4; ++i) v[i] = LD128(rs, base + i * 16);
      const bool ok = (v[0][1] == tag) & (v[0][3] == tag) & (v[1][1] == tag) & (v[1][3] == tag) & (v[2][1] == tag) & (v[2][3] == tag) & (v[3][1] == tag) & (v[3][3] == tag);
      if (__all(ok)) break;
    }
    STAMP(4);
    float a[8];
#pragma unroll
    for (int j = 0; j < 8; ++j) a[j] = 0;
#pragma unroll
    for (int k = 0; k < 8; ++k) {
      const u32 op = v[k >> 1][(k & 1) * 2];
#pragma unroll
      for (int j = 0; j < 8; ++j) a[j] = dot2(P[k][j], op, a[j]);
    }
    float t[4];
#pragma unroll
    for (int j = 0; j < 4; ++j) t[j] = sw32_add(a[j], a[j + 4]);
    const float u0 = sw16_add(t[0], t[2]), u1 = sw16_add(t[1], t[3]);
    const float z = x8_add(u0, u1, (lane & 8) != 0);
    s_red[hw * 64 + 8 * cc + rr] = z;
    lds_barrier();
    if (hw < 2) {
      const int q = lane & 15, rep = hw * 4 + (lane >> 4);
      f32x4 sum = *(const f32x4*)&s_red[q * 4];
#pragma unroll
      for (int ww = 1; ww < 4; ++ww) sum += *(const f32x4*)&s_red[ww * 64 + q * 4];
      ST128(pack4(sum[0], sum[1], sum[2], sum[3], tag), rs, WS_HP + (u32)(((rep * 8 + g) * 128 + cb * 16 + q) * 16));
    }
    STAMP(5);
    return;
  }

  {
    const int idx = b - 64;
    u32 G[4][8], U[4][8], Dn[4][8];
    {
      const int rr = lane >> 2, cc = lane & 3;
      const size_t o = (size_t)(hw * 128 + 8 * rr) * 224 + idx * 4 + cc;
      u32x4 W[8];
#pragma unroll
      for (int i = 0; i < 8; ++i) W[i] = wg[o + (size_t)i * 224];
      u32x4 X[8];
#pragma unroll
      for (int i = 0; i < 8; ++i) X[i] = wu[o + (size_t)i * 224];
      const int rd = lane >> 4, cd = lane & 15;
      const u32x4* dp = wd + (size_t)(idx * 32 + 8 * rd) * 64 + hw * 16 + cd;
      u32x4 Y[8];
#pragma unroll
      for (int i = 0; i < 8; ++i) Y[i] = dp[(size_t)i * 64];
#pragma unroll
      for (int k = 0; k < 4; ++k)
#pragma unroll
        for (int dw = 0; dw < 4; ++dw) {
          G[k][2 * dw] = plo(W[2 * k + 1][dw], W[2 * k][dw]); G[k][2 * dw + 1] = phi(W[2 * k + 1][dw], W[2 * k][dw]);
          U[k][2 * dw] = plo(X[2 * k + 1][dw], X[2 * k][dw]); U[k][2 * dw + 1] = phi(X[2 * k + 1][dw], X[2 * k][dw]);
          Dn[k][2 * dw] = plo(Y[2 * k + 1][dw], Y[2 * k][dw]); Dn[k][2 * dw + 1] = phi(Y[2 * k + 1][dw], Y[2 * k][dw]);
        }
    }
#pragma unroll
    for (int k = 0; k < 4; ++k)
#pragma unroll
      for (int j = 0; j < 8; ++j) { asm volatile("" : "+v"(G[k][j])); asm volatile("" : "+v"(U[k][j])); asm volatile("" : "+v"(Dn[k][j])); }
    const int pidx = 2 * (lane & 31) + (lane >> 5);
    u32 xw = xin[hw * 64 + pidx];
    asm volatile("" : "+v"(xw));
    lds_barrier();
    const u32 base = WS_HP + (u32)((((idx & 7) * 8 + (lane >> 5)) * 128 + hw * 32 + (lane & 31)) * 16);
    u32x4 v[4];
    for (int spin = 0; spin < SPIN_MAX; ++spin) {
      asm volatile("" ::: "memory");
#pragma unroll
      for (int i = 0; i < 4; ++i) v[i] = LD128(rs, base + i * 4096);
      const bool ok = (tag4(v[0]) == tag) & (tag4(v[1]) == tag) & (tag4(v[2]) == tag) & (tag4(v[3]) == tag);
      if (__all(ok)) break;
    }
    STAMP(4);
    float e[4];
#pragma unroll
    for (int k = 0; k < 4; ++k) e[k] = (up24(v[0][k]) + up24(v[1][k])) + (up24(v[2][k]) + up24(v[3][k]));
    const float at0 = sw32_add(e[0], e[2]), at1 = sw32_add(e[1], e[3]);
    const u32 hp = cvtpk(bflo(xw) + bfr(at0), bfhi(xw) + bfr(at1));
    const float h0 = bflo(hp), h1 = bfhi(hp);
    s_h[hw * 64 + pidx] = hp;
    {
      const float K0 = __builtin_bit_cast(float, __builtin_amdgcn_readfirstlane(__builtin_bit_cast(int, h0)));
      const float d0 = h0 - K0, d1 = h1 - K0;
      const float s1 = wave_sum(d0 + d1), s2 = wave_sum(d0 * d0 + d1 * d1);
      if (lane == 0) *(f32x2*)&s_stat[hw * 2] = f32x2{K0 + s1 * (1.f / 128.f), s2 - s1 * s1 * (1.f / 128.f)};
    }
    lds_barrier();
    float a[16];
    {
      const f32x4 st0 = *(const f32x4*)&s_stat[0], st1 = *(const f32x4*)&s_stat[4];
      const u32x4 hq = *(const u32x4*)&s_h[hw * 64 + 4 * (lane >> 2)];
      const float mean = 0.25f * ((st0[0] + st0[2]) + (st1[0] + st1[2]));
      const float e0 = st0[0] - mean, e1 = st0[2] - mean, e2 = st1[0] - mean, e3 = st1[2] - mean;
      const float M2 = ((st0[1] + st0[3]) + (st1[1] + st1[3])) + 128.f * ((e0 * e0 + e1 * e1) + (e2 * e2 + e3 * e3));
      float m, den;
      ln_consts(mean, fmaxf(M2, 0.f) * (1.f / 512.f), m, den);
#pragma unroll
      for (int j = 0; j < 16; ++j) a[j] = 0;
#pragma unroll
      for (int k = 0; k < 4; ++k) {
        const u32 hn = cvtpk(ln_apply(bflo(hq[k]), m, den), ln_apply(bfhi(hq[k]), m, den));
#pragma unroll
        for (int j = 0; j < 8; ++j) { a[j] = dot2(G[k][j], hn, a[j]); a[8 + j] = dot2(U[k][j], hn, a[8 + j]); }
      }
    }
    {
      float t[8], u[4];
#pragma unroll
      for (int j = 0; j < 8; ++j) t[j] = sw32_add(a[j], a[j + 8]);
#pragma unroll
      for (int j = 0; j < 4; ++j) u[j] = sw16_add(t[j], t[j + 4]);
      const bool b3 = (lane & 8) != 0;
      const float z0 = x8_add(u[0], u[2], b3), z1 = x8_add(u[1], u[3], b3);
      const int col = 8 * (lane & 3) + 4 * ((lane >> 4) & 1) + 2 * ((lane >> 3) & 1);
      const int qi = (lane >> 5) * 32 + col;
      const int slot = 2 * hw + ((lane >> 2) & 1);
      s_red[qi * 8 + slot] = z0;
      s_red[(qi + 1) * 8 + slot] = z1;
    }
    lds_barrier();
    u32 hh2;
    {
      const int ty = (lane >> 3) & 1, col = 8 * (lane >> 4) + (lane & 7);
      const f32x4 r0 = *(const f32x4*)&s_red[(ty * 32 + col) * 8], r1 = *(const f32x4*)&s_red[(ty * 32 + col) * 8 + 4];
      const float val = bfr(((r0[0] + r0[1]) + (r0[2] + r0[3])) + ((r1[0] + r1[1]) + (r1[2] + r1[3])));
      const float oth = dpp<ROR8>(val);
      const float gv = ty ? oth : val, uv = ty ? val : oth;
      const float sg = bfr(__builtin_amdgcn_rcpf(1.f + __builtin_amdgcn_exp2f(-gv * L2E)));
      const float hh = sg * uv;
      hh2 = cvtpk(hh, dpp<XOR1>(hh));
    }
    float dacc[8];
#pragma unroll
    for (int j = 0; j < 8; ++j) dacc[j] = 0;
    {
      const u32 p0 = dppu<NBC + 0>(hh2), p1 = dppu<NBC + 2>(hh2), p2 = dppu<NBC + 4>(hh2), p3 = dppu<NBC + 6>(hh2);
#pragma unroll
      for (int j = 0; j < 8; ++j) { dacc[j] = dot2(Dn[0][j], p0, dacc[j]); dacc[j] = dot2(Dn[1][j], p1, dacc[j]); dacc[j] = dot2(Dn[2][j], p2, dacc[j]); dacc[j] = dot2(Dn[3][j], p3, dacc[j]); }
    }
    float t[4];
#pragma unroll
    for (int j = 0; j < 4; ++j) { t[j] = sw32_add(dacc[j], dacc[j + 4]); t[j] = sw16_add(t[j], t[j]); }
    {
      const int cd = lane & 15;
      const int slice = 8 * hw + (cd >> 1), gi = ((cd & 1) << 1) | (lane >> 5);
      const u32 off = (lane & 16) ? OOB : WS_OP + (u32)((slice * 64 + idx) * 128 + gi * 16);
      ST128(pack4(t[0], t[1], t[2], t[3], tag), rs, off);
    }
    STAMP(5);
  }
}

extern "C" void kernel_launch(void* const* d_in, const int* in_sizes, int n_in, void* d_out, int out_size, void* d_ws, size_t ws_size,
                              hipStream_t stream) {
  (void)in_sizes; (void)n_in; (void)out_size; (void)ws_size;
  fused_decode<<<256, 512, 0, stream>>>((const u32*)d_in[0], (const u32x4*)d_in[1], (const u32x4*)d_in[2], (const u32x4*)d_in[3],
                                        (const u32x4*)d_in[4], (const u32x4*)d_in[6], (const u32x4*)d_in[7], (const u32x4*)d_in[8],
                                        (unsigned short*)d_out, (unsigned char*)d_ws
  );
}
```

```cpp
#include <hip/hip_runtime.h>
#include <stdint.h>

typedef unsigned u32;
typedef u32 u32x4 __attribute__((ext_vector_type(4)));
typedef u32 u32x2 __attribute__((ext_vector_type(2)));
typedef float f32x4 __attribute__((ext_vector_type(4)));
typedef float f32x2 __attribute__((ext_vector_type(2)));
typedef __bf16 bf16x8_t __attribute__((ext_vector_type(8)));
typedef __bf16 bf16x2_t __attribute__((ext_vector_type(2)));

#define DEVI __device__ __forceinline__

constexpr int D = 512, SK = 65536, DH = 128, FF = 1792;
constexpr float L2E = 1.4426950408889634f;
constexpr float SCALE = 0.08838834764831845f;

constexpr u32 WS_EPOCH = 0;
constexpr u32 WS_PG = 0x1000;
constexpr u32 WS_QG = 0x10000;
constexpr u32 WS_OG = 0x30000;
constexpr u32 WS_HP = 0x50000;
constexpr u32 WS_OP = 0x80000;
constexpr u32 WS_IN = 0x100000;
constexpr u32 WS_LOCAL = 0x200000;
constexpr u32 WS_BYTES = 0x400000;
constexpr u32 OOB = 0x7ffffff0u;
constexpr int AUX_SC1 = 16, AUX_NT = 2;
constexpr int SPIN_MAX = 1 << 18;

DEVI float bflo(u32 w) { return __builtin_bit_cast(float, w << 16); }
DEVI float bfhi(u32 w) { return __builtin_bit_cast(float, w & 0xffff0000u); }
DEVI u32 cvtpk(float a, float b) { f32x2 v = {a, b}; return __builtin_bit_cast(u32, __builtin_convertvector(v, bf16x2_t)); }
DEVI float bfr(float a) { return bflo(cvtpk(a, a)); }
DEVI float dot2(u32 a, u32 b, float c) {
  return __builtin_amdgcn_fdot2_f32_bf16(__builtin_bit_cast(bf16x2_t, a), __builtin_bit_cast(bf16x2_t, b), c, false);
}
DEVI f32x4 mfma16(u32x4 a, u32x4 b, f32x4 c) {
  return __builtin_amdgcn_mfma_f32_16x16x32_bf16(__builtin_bit_cast(bf16x8_t, a), __builtin_bit_cast(bf16x8_t, b), c, 0, 0, 0);
}
template <int CTRL> DEVI float dpp(float x) {
  return __builtin_bit_cast(float, __builtin_amdgcn_update_dpp(0, __builtin_bit_cast(int, x), CTRL, 0xf, 0xf, true));
}
template <int CTRL> DEVI u32 dppu(u32 x) { return (u32)__builtin_amdgcn_update_dpp(0, (int)x, CTRL, 0xf, 0xf, true); }
constexpr int ROR8 = 0x128, ROR4 = 0x124, HMIR = 0x141, XOR1 = 0xB1, XOR2 = 0x4E, NBC = 0x150;
DEVI float sw32_add(float a, float b) {
  auto s = __builtin_amdgcn_permlane32_swap(__float_as_uint(a), __float_as_uint(b), false, false);
  return __uint_as_float(s[0]) + __uint_as_float(s[1]);
}
DEVI float sw16_add(float a, float b) {
  auto s = __builtin_amdgcn_permlane16_swap(__float_as_uint(a), __float_as_uint(b), false, false);
  return __uint_as_float(s[0]) + __uint_as_float(s[1]);
}
DEVI float sw32_max(float a) {
  auto s = __builtin_amdgcn_permlane32_swap(__float_as_uint(a), __float_as_uint(a), false, false);
  return fmaxf(__uint_as_float(s[0]), __uint_as_float(s[1]));
}
DEVI float sw16_max(float a) {
  auto s = __builtin_amdgcn_permlane16_swap(__float_as_uint(a), __float_as_uint(a), false, false);
  return fmaxf(__uint_as_float(s[0]), __uint_as_float(s[1]));
}
DEVI float x8_add(float a, float b, bool bit3) {
  const float sa = a + dpp<ROR8>(a), sb = b + dpp<ROR8>(b);
  return bit3 ? sb : sa;
}
DEVI float wave_sum(float v) {
  v += dpp<XOR1>(v); v += dpp<XOR2>(v); v += dpp<HMIR>(v); v += dpp<ROR8>(v);
  v = sw16_add(v, v); v = sw32_add(v, v);
  return v;
}
DEVI void lds_barrier() {
  asm volatile("s_waitcnt lgkmcnt(0)" ::: "memory");
  __builtin_amdgcn_s_barrier();
  asm volatile("" ::: "memory");
}
DEVI u32 pk24(float v, u32 tb) { return ((__float_as_uint(v) + 0x80u) & 0xffffff00u) | tb; }
DEVI u32x4 pack4(float a, float b, float c, float d, u32 tag) {
  return u32x4{pk24(a, tag & 0xff), pk24(b, (tag >> 8) & 0xff), pk24(c, (tag >> 16) & 0xff), pk24(d, tag >> 24)};
}
DEVI u32 tag4(u32x4 v) {
  const u32 t01 = __builtin_amdgcn_perm(v[1], v[0], 0x0c0c0400u);
  const u32 t23 = __builtin_amdgcn_perm(v[3], v[2], 0x04000c0cu);
  return t01 | t23;
}
DEVI float up24(u32 w) { return __uint_as_float(w & 0xffffff00u); }
DEVI u32 plo(u32 hi_row, u32 lo_row) { return __builtin_amdgcn_perm(hi_row, lo_row, 0x05040100u); }
DEVI u32 phi(u32 hi_row, u32 lo_row) { return __builtin_amdgcn_perm(hi_row, lo_row, 0x07060302u); }

#define LD128(rs, off) __builtin_amdgcn_raw_buffer_load_b128(rs, (int)(off), 0, AUX_SC1)
#define ST128(v, rs, off) __builtin_amdgcn_raw_buffer_store_b128(v, rs, (int)(off), 0, AUX_SC1)
#define ST64(v, rs, off) __builtin_amdgcn_raw_buffer_store_b64(v, rs, (int)(off), 0, AUX_SC1)
#define ST128L(v, rs, off) do { if (V_LOC) __builtin_amdgcn_raw_buffer_store_b128(v, rs, (int)(off), (int)WS_LOCAL, 0); ST128(v, rs, off); } while (0)
#define ST64L(v, rs, off) do { if (V_LOC) __builtin_amdgcn_raw_buffer_store_b64(v, rs, (int)(off), (int)WS_LOCAL, 0); ST64(v, rs, off); } while (0)

#define STAMP(i) do { } while (0)
#define FLUSH() do { } while (0)
#define V_NA 64
#define V_NB 59
#define STR2(x) #x
#define STR(x) STR2(x)
#define V_QPIPE 0
#define V_QGAP 12
#define V_MIX 1
#define V_D 0
#define V_M 12
#define V_TAU 60
#define V_GAIN 20
#define V_BSLEEP 8
#define V_PD 3
#define V_PG 5
#define POLLT(N, KIND, SL, rs, off, v, tag, LOC) pollx<N, KIND>(rs, off, v, tag, PR, (LOC) && V_LOC ? WS_LOCAL : 0u)
#define PDECL() PollRegs PR
#define PDRAIN() drain(PR)
#define V_WLATE 0
#define V_WADV4 -5
#define V_WADV5 -5
#define V_LOC 0
#define V_AUXK 2
#define V_AUXV V_AUXK
#define V_AUXQ 2
#define V_NTG 1
#define V_TICKET 0
#define V_WEARLY 0
#define V_KDELAY 0
#define V_PACE 8
constexpr int NTILE = SK / 32;
constexpr int NA = V_NA, NB = V_NB;
#define V_NBH V_NB
#define V_GH 6
#define V_ILV 0
constexpr int NBH = V_NBH;
constexpr int NREST = NTILE - 8 * NA - 7 * NB;
constexpr int NC0 = NREST / 17, NCX0 = NREST % 17;
constexpr int NRESTH = NTILE - 8 * NA - 7 * NBH;
constexpr int NC1 = NRESTH / 17, NCX1 = NRESTH % 17;

DEVI void ln_consts(float mean, float var, float& m, float& rden) {
  m = bfr(mean);
  const float v = bfr(var);
  const float den = bfr(__builtin_amdgcn_sqrtf(bfr(v + 1e-5f)));
  rden = __builtin_amdgcn_rcpf(den);
}
DEVI float ln_apply(float x, float m, float rden) { return bfr(x - m) * rden; }

template <int KIND> DEVI bool tag_ok(u32x4 v, u32 tag) {
  if (KIND == 0) return tag4(v) == tag;
  return (v[1] == tag) & (v[3] == tag);
}
template <int N, int KIND, int SLEEP>
DEVI void poll(const __amdgpu_buffer_rsrc_t rs, u32 (&off)[N], u32x4 (&v)[N], u32 tag) {
  bool done[N];
#pragma unroll
  for (int i = 0; i < N; ++i) { done[i] = off[i] == OOB; v[i] = u32x4{0, 0, 0, 0}; }
  for (int spin = 0; spin < SPIN_MAX; ++spin) {
    asm volatile("" ::: "memory");
    u32x4 t[N];
#pragma unroll
    for (int i = 0; i < N; ++i) t[i] = LD128(rs, done[i] ? OOB : off[i]);
    bool all = true;
#pragma unroll
    for (int i = 0; i < N; ++i) {
      const bool ok = tag_ok<KIND>(t[i], tag) & !done[i];
#pragma unroll
      for (int k = 0; k < 4; ++k) v[i][k] = ok ? t[i][k] : v[i][k];
      done[i] = done[i] | ok;
      all = all & done[i];
    }
    if (__all(all)) break;
    if (SLEEP) __builtin_amdgcn_s_sleep(SLEEP);
  }
}

typedef u32 u32x16 __attribute__((ext_vector_type(16)));
struct PollRegs { u32x16 a, b, c; };
DEVI void drain(PollRegs& P) { asm volatile("s_waitcnt vmcnt(0)" : "+{v[192:207]}"(P.a), "+{v[208:223]}"(P.b), "+{v[224:239]}"(P.c) :: "memory"); }
template <int N, int KIND> DEVI void pollx(const __amdgpu_buffer_rsrc_t rs, u32 (&off)[N], u32x4 (&v)[N], u32 tag, PollRegs& P, u32 so);
template <> DEVI void pollx<4, 0>(const __amdgpu_buffer_rsrc_t rs, u32 (&off)[4], u32x4 (&v)[4], u32 tag, PollRegs& P, u32 so) {
  u32 cnt, t0, t1;
  unsigned long long m;
  u32x16 r = {0, 0, 0, 0, 0, 0, 0, 0, 0, 0, 0, 0, 0, 0, 0, 0};
  asm volatile(
      "s_mov_b32 %[cnt], 0\n\t"
      "buffer_load_dwordx4 v[192:195], %[o0], %[rs], %[so] offen sc1\n\tbuffer_load_dwordx4 v[196:199], %[o1], %[rs], %[so] offen sc1\n\tbuffer_load_dwordx4 v[200:203], %[o2], %[rs], %[so] offen sc1\n\tbuffer_load_dwordx4 v[204:207], %[o3], %[rs], %[so] offen sc1\n\ts_sleep " STR(V_PG) "\n\t"
      "buffer_load_dwordx4 v[208:211], %[o0], %[rs], %[so] offen sc1\n\tbuffer_load_dwordx4 v[212:215], %[o1], %[rs], %[so] offen sc1\n\tbuffer_load_dwordx4 v[216:219], %[o2], %[rs], %[so] offen sc1\n\tbuffer_load_dwordx4 v[220:223], %[o3], %[rs], %[so] offen sc1\n\ts_sleep " STR(V_PG) "\n\t"
      "buffer_load_dwordx4 v[224:227], %[o0], %[rs], 0 offen sc1\n\tbuffer_load_dwordx4 v[228:231], %[o1], %[rs], 0 offen sc1\n\tbuffer_load_dwordx4 v[232:235], %[o2], %[rs], 0 offen sc1\n\tbuffer_load_dwordx4 v[236:239], %[o3], %[rs], 0 offen sc1\n\t"
      "PL_%=:\n\t"
      "s_waitcnt vmcnt(8)\n\tv_perm_b32 %[t0], v193, v192, %[sa]\n\tv_perm_b32 %[t1], v195, v194, %[sb]\n\tv_or_b32 %[t0], %[t0], %[t1]\n\tv_cmp_eq_u32 vcc, %[tag], %[t0]\n\tv_cndmask_b32 v240, v240, v192, vcc\n\tv_cndmask_b32 v241, v241, v193, vcc\n\tv_cndmask_b32 v242, v242, v194, vcc\n\tv_cndmask_b32 v243, v243, v195, vcc\n\tv_cndmask_b32 %[o0], %[o0], %[oob], vcc\n\tv_perm_b32 %[t0], v197, v196, %[sa]\n\tv_perm_b32 %[t1], v199, v198, %[sb]\n\tv_or_b32 %[t0], %[t0], %[t1]\n\tv_cmp_eq_u32 vcc, %[tag], %[t0]\n\tv_cndmask_b32 v244, v244, v196, vcc\n\tv_cndmask_b32 v245, v245, v197, vcc\n\tv_cndmask_b32 v246, v246, v198, vcc\n\tv_cndmask_b32 v247, v247, v199, vcc\n\tv_cndmask_b32 %[o1], %[o1], %[oob], vcc\n\tv_perm_b32 %[t0], v201, v200, %[sa]\n\tv_perm_b32 %[t1], v203, v202, %[sb]\n\tv_or_b32 %[t0], %[t0], %[t1]\n\tv_cmp_eq_u32 vcc, %[tag], %[t0]\n\tv_cndmask_b32 v248, v248, v200, vcc\n\tv_cndmask_b32 v249, v249, v201, vcc\n\tv_cndmask_b32 v250, v250, v202, vcc\n\tv_cndmask_b32 v251, v251, v203, vcc\n\tv_cndmask_b32 %[o2], %[o2], %[oob], vcc\n\tv_perm_b32 %[t0], v205, v204, %[sa]\n\tv_perm_b32 %[t1], v207, v206, %[sb]\n\tv_or_b32 %[t0], %[t0], %[t1]\n\tv_cmp_eq_u32 vcc, %[tag], %[t0]\n\tv_cndmask_b32 v252, v252, v204, vcc\n\tv_cndmask_b32 v253, v253, v205, vcc\n\tv_cndmask_b32 v254, v254, v206, vcc\n\tv_cndmask_b32 v255, v255, v207, vcc\n\tv_cndmask_b32 %[o3], %[o3], %[oob], vcc\n\tv_and_b32 %[t0], %[o0], %[o1]\n\tv_and_b32 %[t1], %[o2], %[o3]\n\tv_and_b32 %[t0], %[t0], %[t1]\n\tv_cmp_ne_u32 vcc, %[oob], %[t0]\n\ts_cbranch_vccz PE_%=\n\tbuffer_load_dwordx4 v[192:195], %[o0], %[rs], %[so] offen sc1\n\tbuffer_load_dwordx4 v[196:199], %[o1], %[rs], %[so] offen sc1\n\tbuffer_load_dwordx4 v[200:203], %[o2], %[rs], %[so] offen sc1\n\tbuffer_load_dwordx4 v[204:207], %[o3], %[rs], %[so] offen sc1\n\t"
      "s_waitcnt vmcnt(8)\n\tv_perm_b32 %[t0], v209, v208, %[sa]\n\tv_perm_b32 %[t1], v211, v210, %[sb]\n\tv_or_b32 %[t0], %[t0], %[t1]\n\tv_cmp_eq_u32 vcc, %[tag], %[t0]\n\tv_cndmask_b32 v240, v240, v208, vcc\n\tv_cndmask_b32 v241, v241, v209, vcc\n\tv_cndmask_b32 v242, v242, v210, vcc\n\tv_cndmask_b32 v243, v243, v211, vcc\n\tv_cndmask_b32 %[o0], %[o0], %[oob], vcc\n\tv_perm_b32 %[t0], v213, v212, %[sa]\n\tv_perm_b32 %[t1], v215, v214, %[sb]\n\tv_or_b32 %[t0], %[t0], %[t1]\n\tv_cmp_eq_u32 vcc, %[tag], %[t0]\n\tv_cndmask_b32 v244, v244, v212, vcc\n\tv_cndmask_b32 v245, v245, v213, vcc\n\tv_cndmask_b32 v246, v246, v214, vcc\n\tv_cndmask_b32 v247, v247, v215, vcc\n\tv_cndmask_b32 %[o1], %[o1], %[oob], vcc\n\tv_perm_b32 %[t0], v217, v216, %[sa]\n\tv_perm_b32 %[t1], v219, v218, %[sb]\n\tv_or_b32 %[t0], %[t0], %[t1]\n\tv_cmp_eq_u32 vcc, %[tag], %[t0]\n\tv_cndmask_b32 v248, v248, v216, vcc\n\tv_cndmask_b32 v249, v249, v217, vcc\n\tv_cndmask_b32 v250, v250, v218, vcc\n\tv_cndmask_b32 v251, v251, v219, vcc\n\tv_cndmask_b32 %[o2], %[o2], %[oob], vcc\n\tv_perm_b32 %[t0], v221, v220, %[sa]\n\tv_perm_b32 %[t1], v223, v222, %[sb]\n\tv_or_b32 %[t0], %[t0], %[t1]\n\tv_cmp_eq_u32 vcc, %[tag], %[t0]\n\tv_cndmask_b32 v252, v252, v220, vcc\n\tv_cndmask_b32 v253, v253, v221, vcc\n\tv_cndmask_b32 v254, v254, v222, vcc\n\tv_cndmask_b32 v255, v255, v223, vcc\n\tv_cndmask_b32 %[o3], %[o3], %[oob], vcc\n\tv_and_b32 %[t0], %[o0], %[o1]\n\tv_and_b32 %[t1], %[o2], %[o3]\n\tv_and_b32 %[t0], %[t0], %[t1]\n\tv_cmp_ne_u32 vcc, %[oob], %[t0]\n\ts_cbranch_vccz PE_%=\n\tbuffer_load_dwordx4 v[208:211], %[o0], %[rs], %[so] offen sc1\n\tbuffer_load_dwordx4 v[212:215], %[o1], %[rs], %[so] offen sc1\n\tbuffer_load_dwordx4 v[216:219], %[o2], %[rs], %[so] offen sc1\n\tbuffer_load_dwordx4 v[220:223], %[o3], %[rs], %[so] offen sc1\n\t"
      "s_waitcnt vmcnt(8)\n\tv_perm_b32 %[t0], v225, v224, %[sa]\n\tv_perm_b32 %[t1], v227, v226, %[sb]\n\tv_or_b32 %[t0], %[t0], %[t1]\n\tv_cmp_eq_u32 vcc, %[tag], %[t0]\n\tv_cndmask_b32 v240, v240, v224, vcc\n\tv_cndmask_b32 v241, v241, v225, vcc\n\tv_cndmask_b32 v242, v242, v226, vcc\n\tv_cndmask_b32 v243, v243, v227, vcc\n\tv_cndmask_b32 %[o0], %[o0], %[oob], vcc\n\tv_perm_b32 %[t0], v229, v228, %[sa]\n\tv_perm_b32 %[t1], v231, v230, %[sb]\n\tv_or_b32 %[t0], %[t0], %[t1]\n\tv_cmp_eq_u32 vcc, %[tag], %[t0]\n\tv_cndmask_b32 v244, v244, v228, vcc\n\tv_cndmask_b32 v245, v245, v229, vcc\n\tv_cndmask_b32 v246, v246, v230, vcc\n\tv_cndmask_b32 v247, v247, v231, vcc\n\tv_cndmask_b32 %[o1], %[o1], %[oob], vcc\n\tv_perm_b32 %[t0], v233, v232, %[sa]\n\tv_perm_b32 %[t1], v235, v234, %[sb]\n\tv_or_b32 %[t0], %[t0], %[t1]\n\tv_cmp_eq_u32 vcc, %[tag], %[t0]\n\tv_cndmask_b32 v248, v248, v232, vcc\n\tv_cndmask_b32 v249, v249, v233, vcc\n\tv_cndmask_b32 v250, v250, v234, vcc\n\tv_cndmask_b32 v251, v251, v235, vcc\n\tv_cndmask_b32 %[o2], %[o2], %[oob], vcc\n\tv_perm_b32 %[t0], v237, v236, %[sa]\n\tv_perm_b32 %[t1], v239, v238, %[sb]\n\tv_or_b32 %[t0], %[t0], %[t1]\n\tv_cmp_eq_u32 vcc, %[tag], %[t0]\n\tv_cndmask_b32 v252, v252, v236, vcc\n\tv_cndmask_b32 v253, v253, v237, vcc\n\tv_cndmask_b32 v254, v254, v238, vcc\n\tv_cndmask_b32 v255, v255, v239, vcc\n\tv_cndmask_b32 %[o3], %[o3], %[oob], vcc\n\tv_and_b32 %[t0], %[o0], %[o1]\n\tv_and_b32 %[t1], %[o2], %[o3]\n\tv_and_b32 %[t0], %[t0], %[t1]\n\tv_cmp_ne_u32 vcc, %[oob], %[t0]\n\ts_cbranch_vccz PE_%=\n\tbuffer_load_dwordx4 v[224:227], %[o0], %[rs], 0 offen sc1\n\tbuffer_load_dwordx4 v[228:231], %[o1], %[rs], 0 offen sc1\n\tbuffer_load_dwordx4 v[232:235], %[o2], %[rs], 0 offen sc1\n\tbuffer_load_dwordx4 v[236:239], %[o3], %[rs], 0 offen sc1\n\t"
      "s_add_u32 %[cnt], %[cnt], 1\n\ts_cmp_lt_u32 %[cnt], %[lim]\n\ts_cbranch_scc1 PL_%=\n\t"
      "PE_%=:"
      : [cnt] "=&s"(cnt), [t0] "=&v"(t0), [t1] "=&v"(t1), [m] "=&s"(m), [o0] "+v"(off[0]), [o1] "+v"(off[1]), [o2] "+v"(off[2]), [o3] "+v"(off[3]), "+{v[192:207]}"(P.a), "+{v[208:223]}"(P.b), "+{v[224:239]}"(P.c), "+{v[240:255]}"(r)
      : [rs] "s"(rs), [tag] "v"(tag), [oob] "v"(OOB), [lim] "s"(SPIN_MAX), [sa] "s"(0x0c0c0400u), [sb] "s"(0x04000c0cu), [so] "s"(so)
      : "memory", "vcc", "scc");
#pragma unroll
  for (int i = 0; i < 4; ++i) v[i] = u32x4{r[4 * i], r[4 * i + 1], r[4 * i + 2], r[4 * i + 3]};
}

template <> DEVI void pollx<4, 1>(const __amdgpu_buffer_rsrc_t rs, u32 (&off)[4], u32x4 (&v)[4], u32 tag, PollRegs& P, u32 so) {
  u32 cnt, t0, t1;
  unsigned long long m;
  u32x16 r = {0, 0, 0, 0, 0, 0, 0, 0, 0, 0, 0, 0, 0, 0, 0, 0};
  asm volatile(
      "s_mov_b32 %[cnt], 0\n\t"
      "buffer_load_dwordx4 v[192:195], %[o0], %[rs], %[so] offen sc1\n\tbuffer_load_dwordx4 v[196:199], %[o1], %[rs], %[so] offen sc1\n\tbuffer_load_dwordx4 v[200:203], %[o2], %[rs], %[so] offen sc1\n\tbuffer_load_dwordx4 v[204:207], %[o3], %[rs], %[so] offen sc1\n\ts_sleep " STR(V_PG) "\n\t"
      "buffer_load_dwordx4 v[208:211], %[o0], %[rs], %[so] offen sc1\n\tbuffer_load_dwordx4 v[212:215], %[o1], %[rs], %[so] offen sc1\n\tbuffer_load_dwordx4 v[216:219], %[o2], %[rs], %[so] offen sc1\n\tbuffer_load_dwordx4 v[220:223], %[o3], %[rs], %[so] offen sc1\n\ts_sleep " STR(V_PG) "\n\t"
      "buffer_load_dwordx4 v[224:227], %[o0], %[rs], 0 offen sc1\n\tbuffer_load_dwordx4 v[228:231], %[o1], %[rs], 0 offen sc1\n\tbuffer_load_dwordx4 v[232:235], %[o2], %[rs], 0 offen sc1\n\tbuffer_load_dwordx4 v[236:239], %[o3], %[rs], 0 offen sc1\n\t"
      "PL_%=:\n\t"
      "s_waitcnt vmcnt(8)\n\tv_cmp_eq_u32 vcc, %[tag], v193\n\tv_cmp_eq_u32 %[m], %[tag], v195\n\ts_and_b64 vcc, vcc, %[m]\n\tv_cndmask_b32 v240, v240, v192, vcc\n\tv_cndmask_b32 v241, v241, v193, vcc\n\tv_cndmask_b32 v242, v242, v194, vcc\n\tv_cndmask_b32 v243, v243, v195, vcc\n\tv_cndmask_b32 %[o0], %[o0], %[oob], vcc\n\tv_cmp_eq_u32 vcc, %[tag], v197\n\tv_cmp_eq_u32 %[m], %[tag], v199\n\ts_and_b64 vcc, vcc, %[m]\n\tv_cndmask_b32 v244, v244, v196, vcc\n\tv_cndmask_b32 v245, v245, v197, vcc\n\tv_cndmask_b32 v246, v246, v198, vcc\n\tv_cndmask_b32 v247, v247, v199, vcc\n\tv_cndmask_b32 %[o1], %[o1], %[oob], vcc\n\tv_cmp_eq_u32 vcc, %[tag], v201\n\tv_cmp_eq_u32 %[m], %[tag], v203\n\ts_and_b64 vcc, vcc, %[m]\n\tv_cndmask_b32 v248, v248, v200, vcc\n\tv_cndmask_b32 v249, v249, v201, vcc\n\tv_cndmask_b32 v250, v250, v202, vcc\n\tv_cndmask_b32 v251, v251, v203, vcc\n\tv_cndmask_b32 %[o2], %[o2], %[oob], vcc\n\tv_cmp_eq_u32 vcc, %[tag], v205\n\tv_cmp_eq_u32 %[m], %[tag], v207\n\ts_and_b64 vcc, vcc, %[m]\n\tv_cndmask_b32 v252, v252, v204, vcc\n\tv_cndmask_b32 v253, v253, v205, vcc\n\tv_cndmask_b32 v254, v254, v206, vcc\n\tv_cndmask_b32 v255, v255, v207, vcc\n\tv_cndmask_b32 %[o3], %[o3], %[oob], vcc\n\tv_and_b32 %[t0], %[o0], %[o1]\n\tv_and_b32 %[t1], %[o2], %[o3]\n\tv_and_b32 %[t0], %[t0], %[t1]\n\tv_cmp_ne_u32 vcc, %[oob], %[t0]\n\ts_cbranch_vccz PE_%=\n\tbuffer_load_dwordx4 v[192:195], %[o0], %[rs], %[so] offen sc1\n\tbuffer_load_dwordx4 v[196:199], %[o1], %[rs], %[so] offen sc1\n\tbuffer_load_dwordx4 v[200:203], %[o2], %[rs], %[so] offen sc1\n\tbuffer_load_dwordx4 v[204:207], %[o3], %[rs], %[so] offen sc1\n\t"
      "s_waitcnt vmcnt(8)\n\tv_cmp_eq_u32 vcc, %[tag], v209\n\tv_cmp_eq_u32 %[m], %[tag], v211\n\ts_and_b64 vcc, vcc, %[m]\n\tv_cndmask_b32 v240, v240, v208, vcc\n\tv_cndmask_b32 v241, v241, v209, vcc\n\tv_cndmask_b32 v242, v242, v210, vcc\n\tv_cndmask_b32 v243, v243, v211, vcc\n\tv_cndmask_b32 %[o0], %[o0], %[oob], vcc\n\tv_cmp_eq_u32 vcc, %[tag], v213\n\tv_cmp_eq_u32 %[m], %[tag], v215\n\ts_and_b64 vcc, vcc, %[m]\n\tv_cndmask_b32 v244, v244, v212, vcc\n\tv_cndmask_b32 v245, v245, v213, vcc\n\tv_cndmask_b32 v246, v246, v214, vcc\n\tv_cndmask_b32 v247, v247, v215, vcc\n\tv_cndmask_b32 %[o1], %[o1], %[oob], vcc\n\tv_cmp_eq_u32 vcc, %[tag], v217\n\tv_cmp_eq_u32 %[m], %[tag], v219\n\ts_and_b64 vcc, vcc, %[m]\n\tv_cndmask_b32 v248, v248, v216, vcc\n\tv_cndmask_b32 v249, v249, v217, vcc\n\tv_cndmask_b32 v250, v250, v218, vcc\n\tv_cndmask_b32 v251, v251, v219, vcc\n\tv_cndmask_b32 %[o2], %[o2], %[oob], vcc\n\tv_cmp_eq_u32 vcc, %[tag], v221\n\tv_cmp_eq_u32 %[m], %[tag], v223\n\ts_and_b64 vcc, vcc, %[m]\n\tv_cndmask_b32 v252, v252, v220, vcc\n\tv_cndmask_b32 v253, v253, v221, vcc\n\tv_cndmask_b32 v254, v254, v222, vcc\n\tv_cndmask_b32 v255, v255, v223, vcc\n\tv_cndmask_b32 %[o3], %[o3], %[oob], vcc\n\tv_and_b32 %[t0], %[o0], %[o1]\n\tv_and_b32 %[t1], %[o2], %[o3]\n\tv_and_b32 %[t0], %[t0], %[t1]\n\tv_cmp_ne_u32 vcc, %[oob], %[t0]\n\ts_cbranch_vccz PE_%=\n\tbuffer_load_dwordx4 v[208:211], %[o0], %[rs], %[so] offen sc1\n\tbuffer_load_dwordx4 v[212:215], %[o1], %[rs], %[so] offen sc1\n\tbuffer_load_dwordx4 v[216:219], %[o2], %[rs], %[so] offen sc1\n\tbuffer_load_dwordx4 v[220:223], %[o3], %[rs], %[so] offen sc1\n\t"
      "s_waitcnt vmcnt(8)\n\tv_cmp_eq_u32 vcc, %[tag], v225\n\tv_cmp_eq_u32 %[m], %[tag], v227\n\ts_and_b64 vcc, vcc, %[m]\n\tv_cndmask_b32 v240, v240, v224, vcc\n\tv_cndmask_b32 v241, v241, v225, vcc\n\tv_cndmask_b32 v242, v242, v226, vcc\n\tv_cndmask_b32 v243, v243, v227, vcc\n\tv_cndmask_b32 %[o0], %[o0], %[oob], vcc\n\tv_cmp_eq_u32 vcc, %[tag], v229\n\tv_cmp_eq_u32 %[m], %[tag], v231\n\ts_and_b64 vcc, vcc, %[m]\n\tv_cndmask_b32 v244, v244, v228, vcc\n\tv_cndmask_b32 v245, v245, v229, vcc\n\tv_cndmask_b32 v246, v246, v230, vcc\n\tv_cndmask_b32 v247, v247, v231, vcc\n\tv_cndmask_b32 %[o1], %[o1], %[oob], vcc\n\tv_cmp_eq_u32 vcc, %[tag], v233\n\tv_cmp_eq_u32 %[m], %[tag], v235\n\ts_and_b64 vcc, vcc, %[m]\n\tv_cndmask_b32 v248, v248, v232, vcc\n\tv_cndmask_b32 v249, v249, v233, vcc\n\tv_cndmask_b32 v250, v250, v234, vcc\n\tv_cndmask_b32 v251, v251, v235, vcc\n\tv_cndmask_b32 %[o2], %[o2], %[oob], vcc\n\tv_cmp_eq_u32 vcc, %[tag], v237\n\tv_cmp_eq_u32 %[m], %[tag], v239\n\ts_and_b64 vcc, vcc, %[m]\n\tv_cndmask_b32 v252, v252, v236, vcc\n\tv_cndmask_b32 v253, v253, v237, vcc\n\tv_cndmask_b32 v254, v254, v238, vcc\n\tv_cndmask_b32 v255, v255, v239, vcc\n\tv_cndmask_b32 %[o3], %[o3], %[oob], vcc\n\tv_and_b32 %[t0], %[o0], %[o1]\n\tv_and_b32 %[t1], %[o2], %[o3]\n\tv_and_b32 %[t0], %[t0], %[t1]\n\tv_cmp_ne_u32 vcc, %[oob], %[t0]\n\ts_cbranch_vccz PE_%=\n\tbuffer_load_dwordx4 v[224:227], %[o0], %[rs], 0 offen sc1\n\tbuffer_load_dwordx4 v[228:231], %[o1], %[rs], 0 offen sc1\n\tbuffer_load_dwordx4 v[232:235], %[o2], %[rs], 0 offen sc1\n\tbuffer_load_dwordx4 v[236:239], %[o3], %[rs], 0 offen sc1\n\t"
      "s_add_u32 %[cnt], %[cnt], 1\n\ts_cmp_lt_u32 %[cnt], %[lim]\n\ts_cbranch_scc1 PL_%=\n\t"
      "PE_%=:"
      : [cnt] "=&s"(cnt), [t0] "=&v"(t0), [t1] "=&v"(t1), [m] "=&s"(m), [o0] "+v"(off[0]), [o1] "+v"(off[1]), [o2] "+v"(off[2]), [o3] "+v"(off[3]), "+{v[192:207]}"(P.a), "+{v[208:223]}"(P.b), "+{v[224:239]}"(P.c), "+{v[240:255]}"(r)
      : [rs] "s"(rs), [tag] "v"(tag), [oob] "v"(OOB), [lim] "s"(SPIN_MAX), [sa] "s"(0x0c0c0400u), [sb] "s"(0x04000c0cu), [so] "s"(so)
      : "memory", "vcc", "scc");
#pragma unroll
  for (int i = 0; i < 4; ++i) v[i] = u32x4{r[4 * i], r[4 * i + 1], r[4 * i + 2], r[4 * i + 3]};
}

template <> DEVI void pollx<2, 1>(const __amdgpu_buffer_rsrc_t rs, u32 (&off)[2], u32x4 (&v)[2], u32 tag, PollRegs& P, u32 so) {
  u32 cnt, t0, t1;
  unsigned long long m;
  u32x16 r = {0, 0, 0, 0, 0, 0, 0, 0, 0, 0, 0, 0, 0, 0, 0, 0};
  asm volatile(
      "s_mov_b32 %[cnt], 0\n\t"
      "buffer_load_dwordx4 v[192:195], %[o0], %[rs], %[so] offen sc1\n\tbuffer_load_dwordx4 v[196:199], %[o1], %[rs], %[so] offen sc1\n\ts_sleep " STR(V_PG) "\n\t"
      "buffer_load_dwordx4 v[208:211], %[o0], %[rs], %[so] offen sc1\n\tbuffer_load_dwordx4 v[212:215], %[o1], %[rs], %[so] offen sc1\n\ts_sleep " STR(V_PG) "\n\t"
      "buffer_load_dwordx4 v[224:227], %[o0], %[rs], 0 offen sc1\n\tbuffer_load_dwordx4 v[228:231], %[o1], %[rs], 0 offen sc1\n\t"
      "PL_%=:\n\t"
      "s_waitcnt vmcnt(4)\n\tv_cmp_eq_u32 vcc, %[tag], v193\n\tv_cmp_eq_u32 %[m], %[tag], v195\n\ts_and_b64 vcc, vcc, %[m]\n\tv_cndmask_b32 v240, v240, v192, vcc\n\tv_cndmask_b32 v241, v241, v193, vcc\n\tv_cndmask_b32 v242, v242, v194, vcc\n\tv_cndmask_b32 v243, v243, v195, vcc\n\tv_cndmask_b32 %[o0], %[o0], %[oob], vcc\n\tv_cmp_eq_u32 vcc, %[tag], v197\n\tv_cmp_eq_u32 %[m], %[tag], v199\n\ts_and_b64 vcc, vcc, %[m]\n\tv_cndmask_b32 v244, v244, v196, vcc\n\tv_cndmask_b32 v245, v245, v197, vcc\n\tv_cndmask_b32 v246, v246, v198, vcc\n\tv_cndmask_b32 v247, v247, v199, vcc\n\tv_cndmask_b32 %[o1], %[o1], %[oob], vcc\n\tv_and_b32 %[t0], %[o0], %[o1]\n\tv_cmp_ne_u32 vcc, %[oob], %[t0]\n\ts_cbranch_vccz PE_%=\n\tbuffer_load_dwordx4 v[192:195], %[o0], %[rs], %[so] offen sc1\n\tbuffer_load_dwordx4 v[196:199], %[o1], %[rs], %[so] offen sc1\n\t"
      "s_waitcnt vmcnt(4)\n\tv_cmp_eq_u32 vcc, %[tag], v209\n\tv_cmp_eq_u32 %[m], %[tag], v211\n\ts_and_b64 vcc, vcc, %[m]\n\tv_cndmask_b32 v240, v240, v208, vcc\n\tv_cndmask_b32 v241, v241, v209, vcc\n\tv_cndmask_b32 v242, v242, v210, vcc\n\tv_cndmask_b32 v243, v243, v211, vcc\n\tv_cndmask_b32 %[o0], %[o0], %[oob], vcc\n\tv_cmp_eq_u32 vcc, %[tag], v213\n\tv_cmp_eq_u32 %[m], %[tag], v215\n\ts_and_b64 vcc, vcc, %[m]\n\tv_cndmask_b32 v244, v244, v212, vcc\n\tv_cndmask_b32 v245, v245, v213, vcc\n\tv_cndmask_b32 v246, v246, v214, vcc\n\tv_cndmask_b32 v247, v247, v215, vcc\n\tv_cndmask_b32 %[o1], %[o1], %[oob], vcc\n\tv_and_b32 %[t0], %[o0], %[o1]\n\tv_cmp_ne_u32 vcc, %[oob], %[t0]\n\ts_cbranch_vccz PE_%=\n\tbuffer_load_dwordx4 v[208:211], %[o0], %[rs], %[so] offen sc1\n\tbuffer_load_dwordx4 v[212:215], %[o1], %[rs], %[so] offen sc1\n\t"
      "s_waitcnt vmcnt(4)\n\tv_cmp_eq_u32 vcc, %[tag], v225\n\tv_cmp_eq_u32 %[m], %[tag], v227\n\ts_and_b64 vcc, vcc, %[m]\n\tv_cndmask_b32 v240, v240, v224, vcc\n\tv_cndmask_b32 v241, v241, v225, vcc\n\tv_cndmask_b32 v242, v242, v226, vcc\n\tv_cndmask_b32 v243, v243, v227, vcc\n\tv_cndmask_b32 %[o0], %[o0], %[oob], vcc\n\tv_cmp_eq_u32 vcc, %[tag], v229\n\tv_cmp_eq_u32 %[m], %[tag], v231\n\ts_and_b64 vcc, vcc, %[m]\n\tv_cndmask_b32 v244, v244, v228, vcc\n\tv_cndmask_b32 v245, v245, v229, vcc\n\tv_cndmask_b32 v246, v246, v230, vcc\n\tv_cndmask_b32 v247, v247, v231, vcc\n\tv_cndmask_b32 %[o1], %[o1], %[oob], vcc\n\tv_and_b32 %[t0], %[o0], %[o1]\n\tv_cmp_ne_u32 vcc, %[oob], %[t0]\n\ts_cbranch_vccz PE_%=\n\tbuffer_load_dwordx4 v[224:227], %[o0], %[rs], 0 offen sc1\n\tbuffer_load_dwordx4 v[228:231], %[o1], %[rs], 0 offen sc1\n\t"
      "s_add_u32 %[cnt], %[cnt], 1\n\ts_cmp_lt_u32 %[cnt], %[lim]\n\ts_cbranch_scc1 PL_%=\n\t"
      "PE_%=:"
      : [cnt] "=&s"(cnt), [t0] "=&v"(t0), [t1] "=&v"(t1), [m] "=&s"(m), [o0] "+v"(off[0]), [o1] "+v"(off[1]), "+{v[192:207]}"(P.a), "+{v[208:223]}"(P.b), "+{v[224:239]}"(P.c), "+{v[240:255]}"(r)
      : [rs] "s"(rs), [tag] "v"(tag), [oob] "v"(OOB), [lim] "s"(SPIN_MAX), [sa] "s"(0x0c0c0400u), [sb] "s"(0x04000c0cu), [so] "s"(so)
      : "memory", "vcc", "scc");
#pragma unroll
  for (int i = 0; i < 2; ++i) v[i] = u32x4{r[4 * i], r[4 * i + 1], r[4 * i + 2], r[4 * i + 3]};
}

template <> DEVI void pollx<1, 0>(const __amdgpu_buffer_rsrc_t rs, u32 (&off)[1], u32x4 (&v)[1], u32 tag, PollRegs& P, u32 so) {
  u32 cnt, t0, t1;
  unsigned long long m;
  u32x16 r = {0, 0, 0, 0, 0, 0, 0, 0, 0, 0, 0, 0, 0, 0, 0, 0};
  asm volatile(
      "s_mov_b32 %[cnt], 0\n\t"
      "buffer_load_dwordx4 v[192:195], %[o0], %[rs], %[so] offen sc1\n\ts_sleep " STR(V_PG) "\n\t"
      "buffer_load_dwordx4 v[208:211], %[o0], %[rs], %[so] offen sc1\n\ts_sleep " STR(V_PG) "\n\t"
      "buffer_load_dwordx4 v[224:227], %[o0], %[rs], 0 offen sc1\n\t"
      "PL_%=:\n\t"
      "s_waitcnt vmcnt(2)\n\tv_perm_b32 %[t0], v193, v192, %[sa]\n\tv_perm_b32 %[t1], v195, v194, %[sb]\n\tv_or_b32 %[t0], %[t0], %[t1]\n\tv_cmp_eq_u32 vcc, %[tag], %[t0]\n\tv_cndmask_b32 v240, v240, v192, vcc\n\tv_cndmask_b32 v241, v241, v193, vcc\n\tv_cndmask_b32 v242, v242, v194, vcc\n\tv_cndmask_b32 v243, v243, v195, vcc\n\tv_cndmask_b32 %[o0], %[o0], %[oob], vcc\n\tv_cmp_ne_u32 vcc, %[oob], %[o0]\n\ts_cbranch_vccz PE_%=\n\tbuffer_load_dwordx4 v[192:195], %[o0], %[rs], %[so] offen sc1\n\t"
      "s_waitcnt vmcnt(2)\n\tv_perm_b32 %[t0], v209, v208, %[sa]\n\tv_perm_b32 %[t1], v211, v210, %[sb]\n\tv_or_b32 %[t0], %[t0], %[t1]\n\tv_cmp_eq_u32 vcc, %[tag], %[t0]\n\tv_cndmask_b32 v240, v240, v208, vcc\n\tv_cndmask_b32 v241, v241, v209, vcc\n\tv_cndmask_b32 v242, v242, v210, vcc\n\tv_cndmask_b32 v243, v243, v211, vcc\n\tv_cndmask_b32 %[o0], %[o0], %[oob], vcc\n\tv_cmp_ne_u32 vcc, %[oob], %[o0]\n\ts_cbranch_vccz PE_%=\n\tbuffer_load_dwordx4 v[208:211], %[o0], %[rs], %[so] offen sc1\n\t"
      "s_waitcnt vmcnt(2)\n\tv_perm_b32 %[t0], v225, v224, %[sa]\n\tv_perm_b32 %[t1], v227, v226, %[sb]\n\tv_or_b32 %[t0], %[t0], %[t1]\n\tv_cmp_eq_u32 vcc, %[tag], %[t0]\n\tv_cndmask_b32 v240, v240, v224, vcc\n\tv_cndmask_b32 v241, v241, v225, vcc\n\tv_cndmask_b32 v242, v242, v226, vcc\n\tv_cndmask_b32 v243, v243, v227, vcc\n\tv_cndmask_b32 %[o0], %[o0], %[oob], vcc\n\tv_cmp_ne_u32 vcc, %[oob], %[o0]\n\ts_cbranch_vccz PE_%=\n\tbuffer_load_dwordx4 v[224:227], %[o0], %[rs], 0 offen sc1\n\t"
      "s_add_u32 %[cnt], %[cnt], 1\n\ts_cmp_lt_u32 %[cnt], %[lim]\n\ts_cbranch_scc1 PL_%=\n\t"
      "PE_%=:"
      : [cnt] "=&s"(cnt), [t0] "=&v"(t0), [t1] "=&v"(t1), [m] "=&s"(m), [o0] "+v"(off[0]), "+{v[192:207]}"(P.a), "+{v[208:223]}"(P.b), "+{v[224:239]}"(P.c), "+{v[240:255]}"(r)
      : [rs] "s"(rs), [tag] "v"(tag), [oob] "v"(OOB), [lim] "s"(SPIN_MAX), [sa] "s"(0x0c0c0400u), [sb] "s"(0x04000c0cu), [so] "s"(so)
      : "memory", "vcc", "scc");
#pragma unroll
  for (int i = 0; i < 1; ++i) v[i] = u32x4{r[4 * i], r[4 * i + 1], r[4 * i + 2], r[4 * i + 3]};
}

template <> DEVI void pollx<1, 1>(const __amdgpu_buffer_rsrc_t rs, u32 (&off)[1], u32x4 (&v)[1], u32 tag, PollRegs& P, u32 so) {
  u32 cnt, t0, t1;
  unsigned long long m;
  u32x16 r = {0, 0, 0, 0, 0, 0, 0, 0, 0, 0, 0, 0, 0, 0, 0, 0};
  asm volatile(
      "s_mov_b32 %[cnt], 0\n\t"
      "buffer_load_dwordx4 v[192:195], %[o0], %[rs], %[so] offen sc1\n\ts_sleep " STR(V_PG) "\n\t"
      "buffer_load_dwordx4 v[208:211], %[o0], %[rs], %[so] offen sc1\n\ts_sleep " STR(V_PG) "\n\t"
      "buffer_load_dwordx4 v[224:227], %[o0], %[rs], 0 offen sc1\n\t"
      "PL_%=:\n\t"
      "s_waitcnt vmcnt(2)\n\tv_cmp_eq_u32 vcc, %[tag], v193\n\tv_cmp_eq_u32 %[m], %[tag], v195\n\ts_and_b64 vcc, vcc, %[m]\n\tv_cndmask_b32 v240, v240, v192, vcc\n\tv_cndmask_b32 v241, v241, v193, vcc\n\tv_cndmask_b32 v242, v242, v194, vcc\n\tv_cndmask_b32 v243, v243, v195, vcc\n\tv_cndmask_b32 %[o0], %[o0], %[oob], vcc\n\tv_cmp_ne_u32 vcc, %[oob], %[o0]\n\ts_cbranch_vccz PE_%=\n\tbuffer_load_dwordx4 v[192:195], %[o0], %[rs], %[so] offen sc1\n\t"
      "s_waitcnt vmcnt(2)\n\tv_cmp_eq_u32 vcc, %[tag], v209\n\tv_cmp_eq_u32 %[m], %[tag], v211\n\ts_and_b64 vcc, vcc, %[m]\n\tv_cndmask_b32 v240, v240, v208, vcc\n\tv_cndmask_b32 v241, v241, v209, vcc\n\tv_cndmask_b32 v242, v242, v210, vcc\n\tv_cndmask_b32 v243, v243, v211, vcc\n\tv_cndmask_b32 %[o0], %[o0], %[oob], vcc\n\tv_cmp_ne_u32 vcc, %[oob], %[o0]\n\ts_cbranch_vccz PE_%=\n\tbuffer_load_dwordx4 v[208:211], %[o0], %[rs], %[so] offen sc1\n\t"
      "s_waitcnt vmcnt(2)\n\tv_cmp_eq_u32 vcc, %[tag], v225\n\tv_cmp_eq_u32 %[m], %[tag], v227\n\ts_and_b64 vcc, vcc, %[m]\n\tv_cndmask_b32 v240, v240, v224, vcc\n\tv_cndmask_b32 v241, v241, v225, vcc\n\tv_cndmask_b32 v242, v242, v226, vcc\n\tv_cndmask_b32 v243, v243, v227, vcc\n\tv_cndmask_b32 %[o0], %[o0], %[oob], vcc\n\tv_cmp_ne_u32 vcc, %[oob], %[o0]\n\ts_cbranch_vccz PE_%=\n\tbuffer_load_dwordx4 v[224:227], %[o0], %[rs], 0 offen sc1\n\t"
      "s_add_u32 %[cnt], %[cnt], 1\n\ts_cmp_lt_u32 %[cnt], %[lim]\n\ts_cbranch_scc1 PL_%=\n\t"
      "PE_%=:"
      : [cnt] "=&s"(cnt), [t0] "=&v"(t0), [t1] "=&v"(t1), [m] "=&s"(m), [o0] "+v"(off[0]), "+{v[192:207]}"(P.a), "+{v[208:223]}"(P.b), "+{v[224:239]}"(P.c), "+{v[240:255]}"(r)
      : [rs] "s"(rs), [tag] "v"(tag), [oob] "v"(OOB), [lim] "s"(SPIN_MAX), [sa] "s"(0x0c0c0400u), [sb] "s"(0x04000c0cu), [so] "s"(so)
      : "memory", "vcc", "scc");
#pragma unroll
  for (int i = 0; i < 1; ++i) v[i] = u32x4{r[4 * i], r[4 * i + 1], r[4 * i + 2], r[4 * i + 3]};
}

#define BAL_EXTRA
#define BAL_PASS
DEVI void balance(const __amdgpu_buffer_rsrc_t rs, u32* s_next, int* s_dyn, int g, int s, int cnt, u32 tag, int lane BAL_EXTRA) {
  for (int spin = 0; spin < SPIN_MAX; ++spin) {
    const int n = __builtin_amdgcn_readfirstlane((int)__hip_atomic_load(s_next, __ATOMIC_RELAXED, __HIP_MEMORY_SCOPE_WORKGROUP));
    if (n >= cnt - V_M) break;
    __builtin_amdgcn_s_sleep(8);
  }
  const u32 now = (u32)__builtin_amdgcn_readfirstlane((int)(u32)__builtin_amdgcn_s_memrealtime());
  STAMP(6);
  ST128((u32x4{now, tag, now, tag}), rs, lane == 0 ? WS_PG + (u32)((g * 32 + s) * 16) : OOB);
  u32 off[1] = {lane < 32 ? WS_PG + (u32)((g * 32 + lane) * 16) : OOB};
  u32x4 v[1];
  poll<1, 1, V_BSLEEP>(rs, off, v, tag);
  STAMP(8);
  int d = (int)(v[0][0] - (u32)__builtin_amdgcn_readfirstlane((int)v[0][0]));
  d = lane < 32 ? max(-(1 << 20), min(d, 1 << 20)) : 0;
  int sum = d;
#pragma unroll
  for (int o = 1; o < 64; o <<= 1) sum += __shfl_xor(sum, o);
  const int dev = max(-V_D * V_TAU, min((sum >> 5) - d, 2 * V_D * V_TAU));
  const int x = lane < 32 ? max(0, V_D * V_TAU * 16 + dev * V_GAIN) + 1 : 0;
  int inc = x;
#pragma unroll
  for (int o = 1; o < 32; o <<= 1) { const int t = __shfl_up(inc, o); inc += lane >= o ? t : 0; }
  const u32 total = (u32)__shfl(inc, 31);
  const u32 lo = (u32)(32 * V_D) * (u32)(inc - x) / total, hi = (u32)(32 * V_D) * (u32)inc / total;
  if (lane == s) {
    __hip_atomic_store(&s_dyn[1], NTILE - 32 * V_D + (int)hi, __ATOMIC_RELAXED, __HIP_MEMORY_SCOPE_WORKGROUP);
    asm volatile("s_waitcnt lgkmcnt(0)" ::: "memory");
    __hip_atomic_store(&s_dyn[0], NTILE - 32 * V_D + (int)lo, __ATOMIC_RELAXED, __HIP_MEMORY_SCOPE_WORKGROUP);
  }
}

__global__ void __launch_bounds__(512, 2)
fused_decode(const u32* __restrict__ xin, const u32x4* kc, const u32x4* vc, const u32x4* __restrict__ wq,
             const u32x4* wo, const u32x4* wg, const u32x4* wu, const u32x4* wd,
             unsigned short* __restrict__ out, unsigned char* __restrict__ ws
) {
  __shared__ __attribute__((aligned(16))) u32 s_q[256];
  __shared__ __attribute__((aligned(16))) float s_mrg[4][4][132];
  __shared__ __attribute__((aligned(16))) float s_red[64 * 8];
  __shared__ __attribute__((aligned(16))) u32 s_h[256];
  __shared__ __attribute__((aligned(16))) float s_stat[8];
  __shared__ u32 s_tag;
  __shared__ u32 s_next;
  __shared__ __attribute__((aligned(8))) int s_dyn[2];

  const int tid = threadIdx.x, lane = tid & 63, w = __builtin_amdgcn_readfirstlane(tid >> 6);
  const int g = blockIdx.x >> 5, s = (blockIdx.x & 7) * 4 + ((blockIdx.x >> 3) & 3), b = s * 8 + g;
  const int nb = g >= V_GH ? NBH : NB, NC = g >= V_GH ? NC1 : NC0, NCX = g >= V_GH ? NCX1 : NCX0;
  const int cnt = (s < 8 ? NA : s < 15 ? nb : NC + (s - 15 < NCX ? 1 : 0)) - V_D;
  const int first = (s < 8 ? s * NA : s < 15 ? 8 * NA + (s - 8) * nb : 8 * NA + 7 * nb + (s - 15) * NC + (s - 15 < NCX ? s - 15 : NCX)) - s * V_D;
  const __amdgpu_buffer_rsrc_t rs = __builtin_amdgcn_make_buffer_rsrc((void*)ws, 0, (int)WS_BYTES, 0x00020000);
  STAMP(0);

  if (w == 4) {
    __builtin_amdgcn_s_setprio(3);
    const int half = lane >> 5, rr = lane & 31;
    const u32x4 x0 = *(const u32x4*)(xin + rr * 8), x1 = *(const u32x4*)(xin + rr * 8 + 4);
    const u32 ep = __builtin_amdgcn_raw_buffer_load_b32(rs, (int)WS_EPOCH, 0, AUX_SC1);
    if (lane == 0) { s_next = 8; s_dyn[0] = -1; s_dyn[1] = -1; }
    const __amdgpu_buffer_rsrc_t rw = __builtin_amdgcn_make_buffer_rsrc((void*)wq, 0, D * 4096 * 2, 0x00020000);
    const int woff = (16 * rr) * 8192 + (g * 64 + s * 2 + half) * 16;
    u32x4 W[16];
#pragma unroll
    for (int i = 0; i < 16; ++i) W[i] = __builtin_amdgcn_raw_buffer_load_b128(rw, woff, i * 8192, V_AUXQ);
    __builtin_amdgcn_sched_barrier(0);
    STAMP(7);
    lds_barrier();
    __builtin_amdgcn_sched_barrier(0);
    const u32 tag = ep + 1 + (ep == 0xffffffffu ? 1u : 0u);
    if (lane == 0) s_tag = tag;
    float f[16];
#pragma unroll
    for (int k = 0; k < 4; ++k) { f[2 * k] = bflo(x0[k]); f[2 * k + 1] = bfhi(x0[k]); f[8 + 2 * k] = bflo(x1[k]); f[8 + 2 * k + 1] = bfhi(x1[k]); }
    float sm = 0;
#pragma unroll
    for (int k = 0; k < 16; ++k) sm += f[k];
    const float mean = wave_sum(sm) * (1.f / 1024.f);
    float sv = 0;
#pragma unroll
    for (int k = 0; k < 16; ++k) { const float d = f[k] - mean; sv += d * d; }
    const float var = wave_sum(sv) * (1.f / 1024.f);
    float m, den;
    ln_consts(mean, var, m, den);
    u32 xp[8];
#pragma unroll
    for (int k = 0; k < 8; ++k) xp[k] = cvtpk(ln_apply(f[2 * k], m, den), ln_apply(f[2 * k + 1], m, den));
    asm volatile("" : "+v"(xp[0]), "+v"(xp[1]), "+v"(xp[2]), "+v"(xp[3]), "+v"(xp[4]), "+v"(xp[5]), "+v"(xp[6]), "+v"(xp[7]) :: "memory");
    __builtin_amdgcn_sched_barrier(0);
#pragma unroll
    for (int i = 0; i < 16; ++i) asm volatile("" : "+v"(W[i]));
    float a[8];
#pragma unroll
    for (int j = 0; j < 8; ++j) a[j] = 0;
#pragma unroll
    for (int k = 0; k < 8; ++k)
#pragma unroll
      for (int dw = 0; dw < 4; ++dw) {
        a[2 * dw] = dot2(plo(W[2 * k + 1][dw], W[2 * k][dw]), xp[k], a[2 * dw]);
        a[2 * dw + 1] = dot2(phi(W[2 * k + 1][dw], W[2 * k][dw]), xp[k], a[2 * dw + 1]);
      }
    float t[4];
#pragma unroll
    for (int j = 0; j < 4; ++j) t[j] = sw16_add(a[j], a[j + 4]);
    const bool b3 = (lane & 8) != 0;
    float u0 = x8_add(t[0], t[2], b3), u1 = x8_add(t[1], t[3], b3);
    u0 += dpp<HMIR>(u0); u1 += dpp<HMIR>(u1);
    u0 += dpp<XOR1>(u0); u1 += dpp<XOR1>(u1);
    u0 += dpp<XOR2>(u0); u1 += dpp<XOR2>(u1);
    const u32x2 qg = {cvtpk(u0, u1), tag};
    const u32 qgo = WS_QG + (u32)((((lane & 7) * 8 + g) * 256 + s * 8 + (lane >> 3)) * 8);
    ST64L(qg, rs, qgo);
    STAMP(1);
    const u32 base = WS_QG + (u32)(((s & 7) * 8 + g) * 2048 + lane * 16);
    u32x4 v[2];
    u32 off[2] = {base, base + 1024};
    PDECL();
    POLLT(2, 1, 0, rs, off, v, tag, 1);
    *(u32x2*)&s_q[lane * 2] = u32x2{v[0][0], v[0][2]};
    *(u32x2*)&s_q[128 + lane * 2] = u32x2{v[1][0], v[1][2]};
    STAMP(2);
    lds_barrier();
    PDRAIN();
  } else if (w > 4) {
    if (b >= 120) { FLUSH(); return; }
    lds_barrier();
    lds_barrier();
  } else {
    const int r = lane >> 4, c = lane & 15;
    const size_t hb = (size_t)g * SK * 16;
    const __amdgpu_buffer_rsrc_t rk = __builtin_amdgcn_make_buffer_rsrc((void*)(kc + hb), 0, SK * 256, 0x00020000);
    const __amdgpu_buffer_rsrc_t rv = __builtin_amdgcn_make_buffer_rsrc((void*)(vc + hb), 0, SK * 256, 0x00020000);
    const int koff = ((4 * (c & 3) + (c >> 2)) * 16 + r) * 16;
    const int voff = lane * 16;
    u32x4 K[2][8], V[2][8];
#define LOADK(bf, tile) { const int so_ = (tile) * 8192; _Pragma("unroll") for (int i = 0; i < 8; ++i) \
      K[bf][i] = __builtin_amdgcn_raw_buffer_load_b128(rk, koff + (i & 3) * 64, so_ + (i >> 2) * 4096, V_AUXK); }
#define LOADV(bf, tile) { const int so_ = (tile) * 8192; _Pragma("unroll") for (int t = 0; t < 8; ++t) \
      V[bf][t] = __builtin_amdgcn_raw_buffer_load_b128(rv, voff + (t & 3) * 1024, so_ + (t >> 2) * 4096, V_AUXV); }
#define PACE() asm volatile("s_waitcnt vmcnt(" STR(V_PACE) ")" ::: "memory")
    lds_barrier();
    if (V_KDELAY) __builtin_amdgcn_s_sleep(V_KDELAY);
    __builtin_amdgcn_sched_barrier(0);
    LOADK(0, first + w);
    PACE();
    LOADV(0, first + w);
    PACE();
    LOADK(1, first + 4 + w);
    PACE();
    LOADV(1, first + 4 + w);
    asm volatile("" ::: "memory");
    __builtin_amdgcn_sched_barrier(0);
    STAMP(9);
    f32x4 acc[8];
#pragma unroll
    for (int j = 0; j < 8; ++j) acc[j] = f32x4{0, 0, 0, 0};
    float m = -INFINITY, l = 0.f;
    lds_barrier();
    STAMP(1);
    u32x4 qf[4];
#pragma unroll
    for (int i = 0; i < 4; ++i) {
      const u32x4 t = *(const u32x4*)&s_q[(c & 3) * 64 + r * 4 + 16 * i];
      qf[i] = (c < 4) ? t : u32x4{0, 0, 0, 0};
    }

#define DYNSTAMP() do { } while (0)
#define TICKET_ISSUE() do { } while (0)
#define TICKET_TAKE() \
      const int nxt = mine < cnt ? first + mine : NTILE; \
      mine += 4
#define TILE(bf, cur, valid)                                                                                          \
    {                                                                                                                 \
      TICKET_ISSUE();                                                                                                 \
      f32x4 sa = {0, 0, 0, 0}, sb = {0, 0, 0, 0};                                                                     \
      if (valid) {                                                                                                    \
        _Pragma("unroll") for (int i = 0; i < 4; ++i) { sa = mfma16(K[bf][i], qf[i], sa); sb = mfma16(K[bf][4 + i], qf[i], sb); } \
      }                                                                                                               \
      asm volatile("" : "+v"(sa), "+v"(sb) :: "memory");                                                              \
      TICKET_TAKE();                                                                                                  \
      cur = nxt;                                                                                                      \
      LOADK(bf, nxt);                                                                                                 \
      asm volatile("" ::: "memory");                                                                                  \
      __builtin_amdgcn_sched_barrier(0);                                                                              \
      if (valid) {                                                                                                    \
        float sc[8];                                                                                                  \
        {                                                                                                             \
          const u32 a0 = cvtpk(sa[0], sa[1]), a1 = cvtpk(sa[2], sa[3]), a2 = cvtpk(sb[0], sb[1]), a3 = cvtpk(sb[2], sb[3]); \
          const u32 b0 = cvtpk(bflo(a0) * SCALE, bfhi(a0) * SCALE), b1 = cvtpk(bflo(a1) * SCALE, bfhi(a1) * SCALE);   \
          const u32 b2 = cvtpk(bflo(a2) * SCALE, bfhi(a2) * SCALE), b3 = cvtpk(bflo(a3) * SCALE, bfhi(a3) * SCALE);   \
          sc[0] = bflo(b0); sc[1] = bfhi(b0); sc[2] = bflo(b1); sc[3] = bfhi(b1);                                     \
          sc[4] = bflo(b2); sc[5] = bfhi(b2); sc[6] = bflo(b3); sc[7] = bfhi(b3);                                     \
        }                                                                                                             \
        float mx = fmaxf(fmaxf(fmaxf(sc[0], sc[1]), fmaxf(sc[2], sc[3])), fmaxf(fmaxf(sc[4], sc[5]), fmaxf(sc[6], sc[7]))); \
        mx = sw32_max(sw16_max(mx));                                                                                  \
        const float mn = fmaxf(m, mx);                                                                                \
        const float alpha = __builtin_amdgcn_exp2f((m - mn) * L2E);                                                   \
        m = mn;                                                                                                       \
        float p[8];                                                                                                   \
        _Pragma("unroll") for (int k = 0; k < 8; ++k) p[k] = __builtin_amdgcn_exp2f((sc[k] - mn) * L2E);              \
        l = l * alpha + (((p[0] + p[1]) + (p[2] + p[3])) + ((p[4] + p[5]) + (p[6] + p[7])));                          \
        const u32x4 pf = {cvtpk(p[0], p[1]), cvtpk(p[2], p[3]), cvtpk(p[4], p[5]), cvtpk(p[6], p[7])};                \
        _Pragma("unroll") for (int j = 0; j < 8; ++j) acc[j] *= alpha;                                                \
        _Pragma("unroll") for (int dw = 0; dw < 4; ++dw) {                                                            \
          u32x4 lo, hi;                                                                                               \
          _Pragma("unroll") for (int u = 0; u < 4; ++u) {                                                             \
            lo[u] = plo(V[bf][2 * u + 1][dw], V[bf][2 * u][dw]);                                                      \
            hi[u] = phi(V[bf][2 * u + 1][dw], V[bf][2 * u][dw]);                                                      \
          }                                                                                                           \
          acc[2 * dw] = mfma16(lo, pf, acc[2 * dw]);                                                                  \
          acc[2 * dw + 1] = mfma16(hi, pf, acc[2 * dw + 1]);                                                          \
        }                                                                                                             \
      }                                                                                                               \
      asm volatile("" : "+v"(acc[0]), "+v"(acc[1]), "+v"(acc[2]), "+v"(acc[3]), "+v"(acc[4]), "+v"(acc[5]), "+v"(acc[6]), "+v"(acc[7]) :: "memory"); \
      LOADV(bf, nxt);                                                                                                 \
      asm volatile("" ::: "memory");                                                                                  \
      __builtin_amdgcn_sched_barrier(0);                                                                              \
    }

    int mine = 8 + w;
    int c0 = first + w, c1 = first + 4 + w;
#pragma unroll 1
    do {
      TILE(0, c0, true);
      const bool v1 = c1 < NTILE;
      TILE(1, c1, v1);
    } while (c0 < NTILE);
    STAMP(2);
    l = sw32_add(l, l);
    l = sw16_add(l, l);
    if (c < 4) {
#pragma unroll
      for (int reg = 0; reg < 4; ++reg) {
        float* dst = &s_mrg[w][c][4 + 8 * (4 * r + reg)];
        *(f32x4*)dst = f32x4{acc[0][reg], acc[1][reg], acc[2][reg], acc[3][reg]};
        *(f32x4*)(dst + 4) = f32x4{acc[4][reg], acc[5][reg], acc[6][reg], acc[7][reg]};
      }
      if (r == 0) *(f32x2*)&s_mrg[w][c][0] = f32x2{m, l};
    }
  }

  if (w < 4) {
    lds_barrier();
    const u32 tag = s_tag;
    {
      f32x2 ml[4], a[4];
#pragma unroll
      for (int ww = 0; ww < 4; ++ww) { ml[ww] = *(const f32x2*)&s_mrg[ww][w][0]; a[ww] = *(const f32x2*)&s_mrg[ww][w][4 + 2 * lane]; }
      const float M = fmaxf(fmaxf(ml[0][0], ml[1][0]), fmaxf(ml[2][0], ml[3][0]));
      float L = 0, a0 = 0, a1 = 0;
#pragma unroll
      for (int ww = 0; ww < 4; ++ww) {
        const float wt = __builtin_amdgcn_exp2f((ml[ww][0] - M) * L2E);
        L += wt * ml[ww][1]; a0 += wt * a[ww][0]; a1 += wt * a[ww][1];
      }
      const u32 off = WS_IN + (u32)(((g * 32 + w * 8 + (lane >> 3)) * 32 + s) * 128 + (lane & 7) * 16);
      const u32x4 gr = pack4(a0, a1, M, L, tag);
      ST128L(gr, rs, off);
    }
    STAMP(3);

    if (b >= 128) {
      if (w < 2) {
        const int slice = ((b - 128) >> 3) * 2 + w;
        const u32 base = WS_IN + (u32)((g * 32 + slice) * 4096 + lane * 16);
        u32 off[4] = {base, base + 1024, base + 2048, base + 3072};
        u32x4 v[4];
        PDECL();
        POLLT(4, 0, 0, rs, off, v, tag, 1);
        STAMP(4);
        float M = fmaxf(fmaxf(up24(v[0][2]), up24(v[1][2])), fmaxf(up24(v[2][2]), up24(v[3][2])));
        M = fmaxf(M, dpp<ROR8>(M));
        M = sw32_max(sw16_max(M));
        float L = 0, a0 = 0, a1 = 0;
#pragma unroll
        for (int i = 0; i < 4; ++i) {
          const float wt = __builtin_amdgcn_exp2f((up24(v[i][2]) - M) * L2E);
          L += wt * up24(v[i][3]); a0 += wt * up24(v[i][0]); a1 += wt * up24(v[i][1]);
        }
        L += dpp<ROR8>(L); a0 += dpp<ROR8>(a0); a1 += dpp<ROR8>(a1);
        L = sw16_add(L, L); a0 = sw16_add(a0, a0); a1 = sw16_add(a1, a1);
        L = sw32_add(L, L); a0 = sw32_add(a0, a0); a1 = sw32_add(a1, a1);
        const float rl = __builtin_amdgcn_rcpf(L);
        const u32x2 og = {cvtpk(a0 * rl, a1 * rl), tag};
        const u32 ogo = WS_OG + (u32)((((lane >> 3) * 8 + g) * 256 + slice * 8 + (lane & 7)) * 8);
        ST64L(og, rs, ogo);
        STAMP(5);
        PDRAIN();
      }
    } else if (b >= 120) {
      const int slice = (b - 120) * 4 + w;
      const int gi = lane & 3, pq = lane >> 2;
      const int col = 16 * slice + 4 * gi + 2 * (lane >> 5) + ((lane >> 4) & 1);
      const float xv = bflo((u32)((const unsigned short*)xin)[col]);
      float hval;
      PDECL();
      {
        u32 off[1] = {(pq < 8) ? WS_HP + (u32)((((slice & 7) * 8 + pq) * 128 + slice * 4 + gi) * 16) : OOB};
        u32x4 hv[1];
        POLLT(1, 0, 2, rs, off, hv, tag, 0);
        const float t0 = sw32_add(up24(hv[0][0]), up24(hv[0][2])), t1 = sw32_add(up24(hv[0][1]), up24(hv[0][3]));
        float t = sw16_add(t0, t1);
        t += dpp<ROR8>(t); t += dpp<ROR4>(t);
        hval = bfr(xv + bfr(t));
      }
      STAMP(4);
      const u32 base = WS_OP + (u32)((slice * 64 + pq) * 128 + gi * 16);
      u32 off[4] = {base, base + 2048, base + 4096, (pq < 8) ? base + 6144 : OOB};
      u32x4 v[4];
      POLLT(4, 0, 0, rs, off, v, tag, 0);
      STAMP(5);
      float e[4];
#pragma unroll
      for (int k = 0; k < 4; ++k) e[k] = (up24(v[0][k]) + up24(v[1][k])) + (up24(v[2][k]) + up24(v[3][k]));
      const float t0 = sw32_add(e[0], e[2]), t1 = sw32_add(e[1], e[3]);
      float t = sw16_add(t0, t1);
      t += dpp<ROR8>(t); t += dpp<ROR4>(t);
      const float o = hval + bfr(t);
      if ((lane & 12) == 0) out[col] = (unsigned short)(cvtpk(o, o) & 0xffffu);
      if (slice == 0 && lane == 0) __builtin_amdgcn_raw_buffer_store_b32(tag, rs, (int)WS_EPOCH, 0, AUX_SC1);
      STAMP(6);
      PDRAIN();
    }
    FLUSH();
    return;
  }

  const int hw = w - 4;
  __builtin_amdgcn_s_setprio(3);
#define LATE_BQ() do { if (V_WEARLY && w > 4) { __builtin_amdgcn_sched_barrier(0); lds_barrier(); __builtin_amdgcn_sched_barrier(0); } } while (0)
  if (b >= 120) {
    const u32 tag = s_tag;
    if (V_D) balance(rs, &s_next, s_dyn, g, s, cnt, tag, lane BAL_PASS);
    asm volatile("s_waitcnt lgkmcnt(0)" ::: "memory");
    FLUSH();
    return;
  }
  if (V_D && hw == 0) balance(rs, &s_next, s_dyn, g, s, cnt, s_tag, lane BAL_PASS);

  if (b < 64) {
    const int cb = b >> 3;
    const int rr = lane >> 3, cc = lane & 7;
    const u32x4* wp = wo + (size_t)(g * 512 + hw * 128 + 16 * rr) * 64 + cb * 8 + cc;
    u32 P[8][8];
    {
      u32x4 W[16];
#pragma unroll
      for (int i = 0; i < 16; ++i) W[i] = __builtin_nontemporal_load(wp + (size_t)i * 64);
      LATE_BQ();
#pragma unroll
      for (int k = 0; k < 8; ++k)
#pragma unroll
        for (int dw = 0; dw < 4; ++dw) { P[k][2 * dw] = plo(W[2 * k + 1][dw], W[2 * k][dw]); P[k][2 * dw + 1] = phi(W[2 * k + 1][dw], W[2 * k][dw]); }
    }
#pragma unroll
    for (int k = 0; k < 8; ++k)
#pragma unroll
      for (int j = 0; j < 8; ++j) asm volatile("" : "+v"(P[k][j]));
    STAMP(3);
    const u32 tag = s_tag;
    lds_barrier();
    const u32 base = WS_OG + (u32)(((cb * 8 + g) * 256 + hw * 64 + rr * 8) * 8);
    u32 off[4] = {base, base + 16, base + 32, base + 48};
    u32x4 v[4];
    PDECL();
    POLLT(4, 1, 1, rs, off, v, tag, 1);
    STAMP(4);
    float a[8];
#pragma unroll
    for (int j = 0; j < 8; ++j) a[j] = 0;
#pragma unroll
    for (int k = 0; k < 8; ++k) {
      const u32 op = v[k >> 1][(k & 1) * 2];
#pragma unroll
      for (int j = 0; j < 8; ++j) a[j] = dot2(P[k][j], op, a[j]);
    }
    float t[4];
#pragma unroll
    for (int j = 0; j < 4; ++j) t[j] = sw32_add(a[j], a[j + 4]);
    const float u0 = sw16_add(t[0], t[2]), u1 = sw16_add(t[1], t[3]);
    const float z = x8_add(u0, u1, (lane & 8) != 0);
    s_red[hw * 64 + 8 * cc + rr] = z;
    lds_barrier();
    if (hw < 2) {
      const int q = lane & 15, rep = hw * 4 + (lane >> 4);
      f32x4 sum = *(const f32x4*)&s_red[q * 4];
#pragma unroll
      for (int ww = 1; ww < 4; ++ww) sum += *(const f32x4*)&s_red[ww * 64 + q * 4];
      ST128(pack4(sum[0], sum[1], sum[2], sum[3], tag), rs, WS_HP + (u32)(((rep * 8 + g) * 128 + cb * 16 + q) * 16));
    }
    STAMP(5);
    PDRAIN();
    FLUSH();
    return;
  }

  {
    const int idx = b - 64;
    u32 G[4][8], U[4][8], Dn[4][8];
    {
      const int rr = lane >> 2, cc = lane & 3;
      const size_t o = (size_t)(hw * 128 + 8 * rr) * 224 + idx * 4 + cc;
      u32x4 W[8];
#pragma unroll
      for (int i = 0; i < 8; ++i) W[i] = V_NTG ? __builtin_nontemporal_load(wg + o + (size_t)i * 224) : wg[o + (size_t)i * 224];
      u32x4 X[8];
#pragma unroll
      for (int i = 0; i < 8; ++i) X[i] = V_NTG ? __builtin_nontemporal_load(wu + o + (size_t)i * 224) : wu[o + (size_t)i * 224];
      const int rd = lane >> 4, cd = lane & 15;
      const u32x4* dp = wd + (size_t)(idx * 32 + 8 * rd) * 64 + hw * 16 + cd;
      u32x4 Y[8];
#pragma unroll
      for (int i = 0; i < 8; ++i) Y[i] = __builtin_nontemporal_load(dp + (size_t)i * 64);
      LATE_BQ();
#pragma unroll
      for (int k = 0; k < 4; ++k)
#pragma unroll
        for (int dw = 0; dw < 4; ++dw) {
          G[k][2 * dw] = plo(W[2 * k + 1][dw], W[2 * k][dw]); G[k][2 * dw + 1] = phi(W[2 * k + 1][dw], W[2 * k][dw]);
          U[k][2 * dw] = plo(X[2 * k + 1][dw], X[2 * k][dw]); U[k][2 * dw + 1] = phi(X[2 * k + 1][dw], X[2 * k][dw]);
          Dn[k][2 * dw] = plo(Y[2 * k + 1][dw], Y[2 * k][dw]); Dn[k][2 * dw + 1] = phi(Y[2 * k + 1][dw], Y[2 * k][dw]);
        }
    }
#pragma unroll
    for (int k = 0; k < 4; ++k)
#pragma unroll
      for (int j = 0; j < 8; ++j) { asm volatile("" : "+v"(G[k][j])); asm volatile("" : "+v"(U[k][j])); asm volatile("" : "+v"(Dn[k][j])); }
    const int pidx = 2 * (lane & 31) + (lane >> 5);
    u32 xw = xin[hw * 64 + pidx];
    asm volatile("" : "+v"(xw));
    STAMP(3);
    const u32 tag = s_tag;
    lds_barrier();
    PDECL();
    {
      u32 off[1] = {lane < 8 ? WS_OG + (u32)((((idx & 7) * 8 + lane) * 256 + ((idx >> 3) * 2 + (hw >> 1)) * 16 + (hw & 1) * 8) * 8) : OOB};
      u32x4 gv[1];
      POLLT(1, 1, 4, rs, off, gv, tag, 0);
    }
    const u32 base = WS_HP + (u32)((((idx & 7) * 8 + (lane >> 5)) * 128 + hw * 32 + (lane & 31)) * 16);
    u32 off[4] = {base, base + 4096, base + 8192, base + 12288};
    u32x4 v[4];
    POLLT(4, 0, 0, rs, off, v, tag, 0);
    STAMP(4);
    float e[4];
#pragma unroll
    for (int k = 0; k < 4; ++k) e[k] = (up24(v[0][k]) + up24(v[1][k])) + (up24(v[2][k]) + up24(v[3][k]));
    const float at0 = sw32_add(e[0], e[2]), at1 = sw32_add(e[1], e[3]);
    const u32 hp = cvtpk(bflo(xw) + bfr(at0), bfhi(xw) + bfr(at1));
    const float h0 = bflo(hp), h1 = bfhi(hp);
    s_h[hw * 64 + pidx] = hp;
    {
      const float K0 = __builtin_bit_cast(float, __builtin_amdgcn_readfirstlane(__builtin_bit_cast(int, h0)));
      const float d0 = h0 - K0, d1 = h1 - K0;
      const float s1 = wave_sum(d0 + d1), s2 = wave_sum(d0 * d0 + d1 * d1);
      if (lane == 0) *(f32x2*)&s_stat[hw * 2] = f32x2{K0 + s1 * (1.f / 128.f), s2 - s1 * s1 * (1.f / 128.f)};
    }
    lds_barrier();
    float a[16];
    {
      const f32x4 st0 = *(const f32x4*)&s_stat[0], st1 = *(const f32x4*)&s_stat[4];
      const u32x4 hq = *(const u32x4*)&s_h[hw * 64 + 4 * (lane >> 2)];
      const float mean = 0.25f * ((st0[0] + st0[2]) + (st1[0] + st1[2]));
      const float e0 = st0[0] - mean, e1 = st0[2] - mean, e2 = st1[0] - mean, e3 = st1[2] - mean;
      const float M2 = ((st0[1] + st0[3]) + (st1[1] + st1[3])) + 128.f * ((e0 * e0 + e1 * e1) + (e2 * e2 + e3 * e3));
      float m, den;
      ln_consts(mean, fmaxf(M2, 0.f) * (1.f / 512.f), m, den);
#pragma unroll
      for (int j = 0; j < 16; ++j) a[j] = 0;
#pragma unroll
      for (int k = 0; k < 4; ++k) {
        const u32 hn = cvtpk(ln_apply(bflo(hq[k]), m, den), ln_apply(bfhi(hq[k]), m, den));
#pragma unroll
        for (int j = 0; j < 8; ++j) { a[j] = dot2(G[k][j], hn, a[j]); a[8 + j] = dot2(U[k][j], hn, a[8 + j]); }
      }
    }
    {
      float t[8], u[4];
#pragma unroll
      for (int j = 0; j < 8; ++j) t[j] = sw32_add(a[j], a[j + 8]);
#pragma unroll
      for (int j = 0; j < 4; ++j) u[j] = sw16_add(t[j], t[j + 4]);
      const bool b3 = (lane & 8) != 0;
      const float z0 = x8_add(u[0], u[2], b3), z1 = x8_add(u[1], u[3], b3);
      const int col = 8 * (lane & 3) + 4 * ((lane >> 4) & 1) + 2 * ((lane >> 3) & 1);
      const int qi = (lane >> 5) * 32 + col;
      const int slot = 2 * hw + ((lane >> 2) & 1);
      s_red[qi * 8 + slot] = z0;
      s_red[(qi + 1) * 8 + slot] = z1;
    }
    lds_barrier();
    u32 hh2;
    {
      const int ty = (lane >> 3) & 1, col = 8 * (lane >> 4) + (lane & 7);
      const f32x4 r0 = *(const f32x4*)&s_red[(ty * 32 + col) * 8], r1 = *(const f32x4*)&s_red[(ty * 32 + col) * 8 + 4];
      const float val = bfr(((r0[0] + r0[1]) + (r0[2] + r0[3])) + ((r1[0] + r1[1]) + (r1[2] + r1[3])));
      const float oth = dpp<ROR8>(val);
      const float gv = ty ? oth : val, uv = ty ? val : oth;
      const float sg = bfr(__builtin_amdgcn_rcpf(1.f + __builtin_amdgcn_exp2f(-gv * L2E)));
      const float hh = sg * uv;
      hh2 = cvtpk(hh, dpp<XOR1>(hh));
    }
    float dacc[8];
#pragma unroll
    for (int j = 0; j < 8; ++j) dacc[j] = 0;
    {
      const u32 p0 = dppu<NBC + 0>(hh2), p1 = dppu<NBC + 2>(hh2), p2 = dppu<NBC + 4>(hh2), p3 = dppu<NBC + 6>(hh2);
#pragma unroll
      for (int j = 0; j < 8; ++j) { dacc[j] = dot2(Dn[0][j], p0, dacc[j]); dacc[j] = dot2(Dn[1][j], p1, dacc[j]); dacc[j] = dot2(Dn[2][j], p2, dacc[j]); dacc[j] = dot2(Dn[3][j], p3, dacc[j]); }
    }
    float t[4];
#pragma unroll
    for (int j = 0; j < 4; ++j) { t[j] = sw32_add(dacc[j], dacc[j + 4]); t[j] = sw16_add(t[j], t[j]); }
    {
      const int cd = lane & 15;
      const int slice = 8 * hw + (cd >> 1), gi = ((cd & 1) << 1) | (lane >> 5);
      const u32 off = (lane & 16) ? OOB : WS_OP + (u32)((slice * 64 + idx) * 128 + gi * 16);
      ST128(pack4(t[0], t[1], t[2], t[3], tag), rs, off);
    }
    STAMP(5);
    PDRAIN();
    FLUSH();
  }
}

extern "C" void kernel_launch(void* const* d_in, const int* in_sizes, int n_in, void* d_out, int out_size, void* d_ws, size_t ws_size,
                              hipStream_t stream) {
  (void)in_sizes; (void)n_in; (void)out_size; (void)ws_size;
  fused_decode<<<256, 512, 0, stream>>>((const u32*)d_in[0], (const u32x4*)d_in[1], (const u32x4*)d_in[2], (const u32x4*)d_in[3],
                                        (const u32x4*)d_in[4], (const u32x4*)d_in[6], (const u32x4*)d_in[7], (const u32x4*)d_in[8],
                                        (unsigned short*)d_out, (unsigned char*)d_ws
  );
}
```
